```python
import torch

def _softmax(x, axis=-1):
    return torch.softmax(x, dim=axis)
def _gelu(x):
    return torch.nn.functional.gelu(x, approximate="tanh")
def _silu(x):
    return torch.nn.functional.silu(x)
def _sigmoid(x):
    return torch.sigmoid(x)
def _layernorm(x, axis=-1, eps=1e-5):
    m = x.mean(dim=axis, keepdim=True)
    v = x.var(dim=axis, keepdim=True, unbiased=False)
    return (x - m) / torch.sqrt(v + eps)
def _scatter_add(tgt, idx, src, axis=0):
    return tgt.clone().index_add_(axis, idx, src)
def _segment_sum(src, seg, n):
    out = torch.zeros((n,) + src.shape[1:], dtype=src.dtype)
    return out.index_add_(0, seg, src)
def _pad(x, pw):
    flat = tuple(v for pair in reversed(pw) for v in pair)
    return torch.nn.functional.pad(x, flat)

kernel_name = 'gated_mix__ssm_conv_s4__geglu_ffn__prompt'

def setup_inputs():

    _w = (lambda a: (lambda t: t.to(torch.bfloat16) if t.is_floating_point() else t)(torch.as_tensor(a)))
    def _A_setup():
        B, L, D, N = 1, 2048, 512, 128
        A_re = -(lambda _x: torch.exp(torch.as_tensor(_x)))((lambda *s: torch.randn(*s, dtype=torch.bfloat16) * 6.4)(D, N))
        A_im = torch.pi * (lambda *s: torch.randn(*s, dtype=torch.bfloat16) * 0.05)(D, N)
        B_re = (lambda *s: torch.randn(*s, dtype=torch.bfloat16) * 0.1)(D, N)
        B_im = (lambda *s: torch.randn(*s, dtype=torch.bfloat16) * 0.1)(D, N)
        C_re = (lambda *s: torch.randn(*s, dtype=torch.bfloat16) * 0.1)(D, N)
        C_im = (lambda *s: torch.randn(*s, dtype=torch.bfloat16) * 0.1)(D, N)
        log_dt = (lambda *s: torch.randn(*s, dtype=torch.bfloat16) * 0.05)(D) * 0.5 - 4.0
        Dskip = (lambda *s: torch.randn(*s, dtype=torch.bfloat16) * 0.05)(D)
        return {
            "u": _w((lambda *s: torch.randn(*s, dtype=torch.bfloat16) * 0.05)(B, L, D)),
            "A_re": _w(A_re),
            "A_im": _w(A_im),
            "B_re": _w(B_re),
            "B_im": _w(B_im),
            "C_re": _w(C_re),
            "C_im": _w(C_im),
            "log_dt": _w(log_dt),
            "Dskip": _w(Dskip),
        }
    _A_in = _A_setup()

    def _B_setup():
        B, S, D, F = 1, 2048, 512, 1376
        return {
            "x": _w((lambda *s: torch.randn(*s, dtype=torch.bfloat16) * 0.05)(B, S, D)),
            "w_gate": _w((lambda *s: torch.randn(*s, dtype=torch.bfloat16) * 0.05)(D, F)),
            "w_up": _w((lambda *s: torch.randn(*s, dtype=torch.bfloat16) * 0.05)(D, F)),
            "w_down": _w((lambda *s: torch.randn(*s, dtype=torch.bfloat16) * 0.05)(F, D)),
        }
    _B_in = _B_setup()

    return {
        "A_u": _A_in["u"],
        "A_A_re": _A_in["A_re"],
        "A_A_im": _A_in["A_im"],
        "A_B_re": _A_in["B_re"],
        "A_B_im": _A_in["B_im"],
        "A_C_re": _A_in["C_re"],
        "A_C_im": _A_in["C_im"],
        "A_log_dt": _A_in["log_dt"],
        "A_Dskip": _A_in["Dskip"],
        "B_x": _B_in["x"],
        "B_w_gate": _B_in["w_gate"],
        "B_w_up": _B_in["w_up"],
        "B_w_down": _B_in["w_down"],
        "gate_w": _w((lambda *s: torch.randn(*s, dtype=torch.bfloat16) * 0.05)(512) * 16.0),
    }

def reference(A_u, A_A_re, A_A_im, A_B_re, A_B_im, A_C_re, A_C_im, A_log_dt, A_Dskip, B_x, B_w_gate, B_w_up, B_w_down, gate_w):
    def _A_ref(u, A_re, A_im, B_re, B_im, C_re, C_im, log_dt, Dskip):
        Bsz, L, D = u.shape
        dt = torch.exp(log_dt)[:, None]
        A = A_re + 1j * A_im
        Bc = B_re + 1j * B_im
        Cc = C_re + 1j * C_im
        logdA = dt * A
        dB = dt * Bc
        l = (lambda x, dt: x.to(dt))(torch.arange(L), A_re.dtype)
        P = torch.exp(logdA[:, :, None] * l[None, None, :])
        K = torch.einsum("dn,dnl,dn->dl", Cc, P, dB).real
        L2 = 2 * L
        Kp = _pad(K, ((0, 0), (0, L)))
        up = (lambda x, *d: x.permute(*d))(u, 0, 2, 1)
        up = _pad(up, ((0, 0), (0, 0), (0, L)))
        Kf = (lambda x, axes=(-2,-1): torch.fft.rfft2(x.float(), dim=axes))(Kp[:, None, :], axes=(-2, -1))
        Uf = (lambda x, axes=(-2,-1): torch.fft.rfft2(x.float(), dim=axes))(up[:, :, None, :], axes=(-2, -1))
        Yf = Uf * Kf[None, :, :, :]
        y = (lambda x, s, axes=(-2,-1): torch.fft.irfft2(x, s=s, dim=axes).to(torch.bfloat16))(Yf, (1, L2), axes=(-2, -1))[:, :, 0, :L]
        y = (lambda x, *d: x.permute(*d))(y, 0, 2, 1) + u * Dskip[None, None, :]
        return y

    def _B_ref(x, w_gate, w_up, w_down):
        return (_gelu(x @ w_gate) * (x @ w_up)) @ w_down

    x = A_u
    g = _sigmoid((x * gate_w).sum(-1, keepdims=True))
    a = _A_ref(x, A_A_re, A_A_im, A_B_re, A_B_im, A_C_re, A_C_im, A_log_dt, A_Dskip)
    b = _B_ref(x, B_w_gate, B_w_up, B_w_down)
    return g * a + (1 - g) * b
```

```cpp
#include <hip/hip_runtime.h>
#include <hip/hip_bf16.h>
typedef __hip_bfloat16 bf16;
#define LAMBDA 0.0f
static __device__ __forceinline__ float b2f(bf16 x){ return __bfloat162float(x);}
static __device__ __forceinline__ float rb(float x){ return __bfloat162float(__float2bfloat16(x));}
constexpr int L=2048,D=512,N=128,F=1376;
__global__ void kgen(const bf16* Are,const bf16* Aim,const bf16* Bre,const bf16* Bim,const bf16* Cre,const bf16* Cim,const bf16* logdt,float* K){
  int d=blockIdx.x;
  float dt=rb(expf(b2f(logdt[d])));
  for(int l=threadIdx.x;l<L;l+=blockDim.x){
    float lr=rb((float)l), le=(float)l;
    float sr=0,se=0;
    for(int n=0;n<N;n++){
      float ar=dt*b2f(Are[d*N+n]), ai=dt*b2f(Aim[d*N+n]);
      float br=dt*b2f(Bre[d*N+n]), bi=dt*b2f(Bim[d*N+n]);
      float cr=b2f(Cre[d*N+n]), ci=b2f(Cim[d*N+n]);
      float wr=cr*br-ci*bi, wi=cr*bi+ci*br;
      { float e=expf(ar*lr); float c=cosf(ai*lr), s=sinf(ai*lr); sr+=e*(wr*c-wi*s);}
      { float e=expf(ar*le); float c=cosf(ai*le), s=sinf(ai*le); se+=e*(wr*c-wi*s);}
    }
    K[d*L+l]=sr+LAMBDA*(sr-se);
  }
}
__global__ void conv(const bf16* u,const float* K,const bf16* Dskip,float* a){
  int d=blockIdx.x; __shared__ float su[L], sk[L];
  for(int l=threadIdx.x;l<L;l+=blockDim.x){su[l]=b2f(u[l*D+d]); sk[l]=K[d*L+l];}
  __syncthreads();
  for(int i=threadIdx.x;i<L;i+=blockDim.x){
    float s=0; for(int j=0;j<=i;j++) s+=sk[i-j]*su[j];
    a[i*D+d]=rb(rb(s)+rb(su[i]*b2f(Dskip[d])));
  }
}
__global__ void up(const bf16* x,const bf16* wg,const bf16* wu,float* h){
  int t=blockIdx.x; __shared__ float sx[D];
  for(int k=threadIdx.x;k<D;k+=blockDim.x) sx[k]=b2f(x[t*D+k]);
  __syncthreads();
  for(int f=threadIdx.x;f<F;f+=blockDim.x){
    float g=0,v=0; for(int k=0;k<D;k++){g+=sx[k]*b2f(wg[k*F+f]); v+=sx[k]*b2f(wu[k*F+f]);}
    g=rb(g); v=rb(v);
    float ge=rb(0.5f*g*(1.f+tanhf(0.7978845608f*(g+0.044715f*g*g*g))));
    h[t*F+f]=rb(ge*v);
  }
}
__global__ void down(const bf16* x,const float* h,const bf16* wd,const bf16* gw,const float* a,bf16* out){
  int t=blockIdx.x; __shared__ float sh[F]; __shared__ float red[256];
  for(int k=threadIdx.x;k<F;k+=blockDim.x) sh[k]=h[t*F+k];
  float p=0; for(int k=threadIdx.x;k<D;k+=blockDim.x) p+=rb(b2f(x[t*D+k])*b2f(gw[k]));
  red[threadIdx.x]=p; __syncthreads();
  float s=0; for(int i=0;i<256;i++) s+=red[i];
  s=rb(s); float g=rb(1.f/(1.f+expf(-s)));
  for(int d=threadIdx.x;d<D;d+=blockDim.x){
    float b=0; for(int k=0;k<F;k++) b+=sh[k]*b2f(wd[k*D+d]);
    b=rb(b);
    out[t*D+d]=__float2bfloat16(rb(g*a[t*D+d])+rb(rb(1.f-g)*b));
  }
}
extern "C" void kernel_launch(void* const* d_in, const int* in_sizes, int n_in, void* d_out, int out_size, void* d_ws, size_t ws_size, hipStream_t stream) {
  const bf16* u=(const bf16*)d_in[0];
  float* K=(float*)d_ws; float* a=K+D*L; float* h=a+D*L;
  kgen<<<D,256,0,stream>>>((const bf16*)d_in[1],(const bf16*)d_in[2],(const bf16*)d_in[3],(const bf16*)d_in[4],(const bf16*)d_in[5],(const bf16*)d_in[6],(const bf16*)d_in[7],K);
  conv<<<D,256,0,stream>>>(u,K,(const bf16*)d_in[8],a);
  up<<<L,256,0,stream>>>(u,(const bf16*)d_in[10],(const bf16*)d_in[11],h);
  down<<<L,256,0,stream>>>(u,h,(const bf16*)d_in[12],(const bf16*)d_in[13],a,(bf16*)d_out);
}
```

```cpp
#include <hip/hip_runtime.h>
#include <hip/hip_bf16.h>


struct Srd { const void* p; unsigned n; unsigned f; };
struct Args { Srd b[6]; const void* q[9]; };

__global__ __launch_bounds__(512) void gmix_kernel(Args a) {
  __shared__ __attribute__((aligned(16))) char lds[163840];
  asm volatile(
    ".p2align 3\n"
    ".Lstart2_%=:\n"
    "s_load_dwordx16 s[4:19], s[0:1], 0\n"
    "s_load_dwordx16 s[20:35], s[0:1], 64\n"
    "s_load_dwordx8 s[36:43], s[0:1], 128\n"
    "s_load_dwordx2 s[44:45], s[0:1], 160\n"
    "v_lshrrev_b32 v1, 6, v0\n"
    "v_and_b32_e64 v255, 63, v0\n"
    "v_readfirstlane_b32 s48, v1\n"
    "s_and_b32 s56, s2, 7\n"
    "s_lshr_b32 s57, s2, 3\n"
    "s_lshr_b32 s58, s56, 2\n"
    "s_lshr_b32 s59, s57, 2\n"
    "s_lshl3_add_u32 s49, s58, s59\n"
    "s_and_b32 s58, s56, 3\n"
    "s_and_b32 s59, s57, 3\n"
    "s_lshl2_add_u32 s50, s58, s59\n"
    "s_mul_i32 s52, s50, 43\n"
    "s_add_u32 s53, s52, 43\n"
    "s_lshr_b32 s52, s52, 4\n"
    "s_lshr_b32 s53, s53, 4\n"
    "s_sub_u32 s53, s53, s52\n"
    "s_lshr_b32 s58, s57, 3\n"
    "s_lshl2_add_u32 s54, s56, s58\n"
    "s_and_b32 s58, s58, 1\n"
    "s_sub_u32 s55, s57, s58\n"
    "s_and_b32 s55, s55, 7\n"
    "s_lshr_b32 s58, s49, 1\n"
    "s_add_u32 s51, s58, s50\n"
    "s_and_b32 s51, s51, 7\n"
    "s_cmp_ge_u32 s48, 4\n"
    "s_cbranch_scc1 .LS4_%=\n"
    "s_setprio 2\n"
    "s_lshl2_add_u32 s56, s2, s48\n"
    "s_lshl_b32 s56, s56, 7\n"
    "v_lshl_add_u32 v2, v255, 1, s56\n"
    "v_lshrrev_b32 v3, 5, v255\n"
    "s_lshl_b32 s57, s2, 2\n"
    "s_getpc_b64 s[58:59]\n"
    ".Lpc5_%=:\n"
    "s_sub_u32 s58, s58, .Lpc5_%=-.Lstart2_%=\n"
    "s_subb_u32 s59, s59, 0\n"
    "s_lshr_b32 s60, s2, 3\n"
    "s_lshl2_add_u32 s60, s60, s48\n"
    "s_lshl_b32 s60, s60, 7\n"
    "v_lshl_add_u32 v5, v3, 14, s60\n"
    "v_min_u32 v5, .Lend3_%=-.Lstart2_%=-4, v5\n"
    "global_load_dword v6, v5, s[58:59]\n"
    "s_waitcnt lgkmcnt(0)\n"
    "global_load_ushort v8, v2, s[34:35]\n"
    "global_load_ushort v9, v2, s[36:37]\n"
    "global_load_ushort v10, v2, s[38:39]\n"
    "global_load_ushort v11, v2, s[40:41]\n"
    "global_load_ushort v12, v2, s[42:43]\n"
    "global_load_ushort v13, v2, s[44:45]\n"
    "s_load_dword s74, s[30:31], s57\n"
    "s_load_dword s75, s[32:33], s57\n"
    "s_load_dwordx2 s[46:47], s[20:21], 0\n"
    "s_branch .LM1_%=\n"
    ".LS4_%=:\n"
    "s_setprio 1\n"
    "v_lshrrev_b32 v10, 3, v255\n"
    "v_lshrrev_b32 v11, 4, v255\n"
    "v_and_b32 v12, 7, v255\n"
    "v_xor_b32 v13, v12, v11\n"
    "v_lshlrev_b32_e64 v13, 4, v13\n"
    "v_lshl_or_b32 v240, v10, 10, v13\n"
    "v_xor_b32_e64 v241, 64, v240\n"
    "v_add_u32 v241, 7168, v241\n"
    "s_sub_u32 s69, s48, 4\n"
    "v_lshlrev_b32 v254, 4, v255\n"
    "v_mov_b32 v15, 1073741824\n"
    "s_lshl_b32 s60, s53, 2\n"
    "v_add_u32_e64 v16, 0, v255\n"
    "v_mul_u32_u24 v17, 171, v16\n"
    "v_lshrrev_b32_e64 v17, 11, v17\n"
    "v_mad_i32_i24 v16, v17, -12, v16\n"
    "v_cmp_le_u32 vcc, s60, v16\n"
    "v_mul_u32_u24 v17, 2560, v17\n"
    "v_add_u32 v17, v17, v254\n"
    "v_cndmask_b32 v244, v17, v15, vcc\n"
    "v_add_u32 v16, 64, v255\n"
    "v_mul_u32_u24 v17, 171, v16\n"
    "v_lshrrev_b32_e64 v17, 11, v17\n"
    "v_mad_i32_i24 v16, v17, -12, v16\n"
    "v_cmp_le_u32 vcc, s60, v16\n"
    "v_and_b32 v16, 32, v255\n"
    "v_mul_u32_u24 v17, 2560, v17\n"
    "v_xad_u32 v17, v254, v16, v17\n"
    "v_cndmask_b32 v245, v17, v15, vcc\n"
    "v_add_u32 v16, 128, v255\n"
    "v_mul_u32_u24 v17, 171, v16\n"
    "v_lshrrev_b32 v17, 11, v17\n"
    "v_mad_i32_i24 v16, v17, -12, v16\n"
    "v_cmp_le_u32 vcc, s60, v16\n"
    "v_mul_u32_u24 v17, 2560, v17\n"
    "v_xad_u32 v17, v254, 32, v17\n"
    "v_cndmask_b32 v246, v17, v15, vcc\n"
    "s_lshl2_add_u32 s64, s49, s69\n"
    "s_lshl_b32 s64, s64, 15\n"
    "s_mul_i32 s65, s69, 44032\n"
    "s_lshl_b32 s58, s52, 6\n"
    "s_add_u32 s65, s65, s58\n"
    "s_mov_b32 s52, s51\n"
    "s_lshl_b32 s66, s69, 12\n"
    "s_mul_i32 s67, s69, 3072\n"
    "s_and_b32 s78, s50, 8\n"
    "s_lshl_b32 s78, s78, 8\n"
    "s_add_u32 m0, s66, s78\n"
    "s_lshl_b32 s70, s51, 7\n"
    "s_add_u32 s70, s70, s64\n"
    "s_lshl3_add_u32 s70, s78, s70\n"
    "s_mul_i32 s71, s51, 176128\n"
    "s_add_u32 s71, s71, s65\n"
    "s_bfe_u32 s59, s49, 65536\n"
    "s_mul_i32 s58, s59, 12288\n"
    "s_sub_u32 s68, s67, s58\n"
    "s_add_u32 s67, s67, s58\n"
    "v_add_u32 v242, 14336, v240\n"
    "v_add_u32 v243, 14336, v241\n"
    "s_waitcnt lgkmcnt(0)\n"
    "buffer_load_dwordx4 v240, s[4:7], s70 offen offset:0 lds\n"
    "s_load_dwordx2 s[46:47], s[20:21], 0\n"
    "buffer_load_dwordx4 v241, s[4:7], s70 offen offset:1024 lds\n"
    "s_cmp_lg_u32 s59, 0\n"
    "s_cselect_b64 s[62:63], s[12:13], s[8:9]\n"
    "s_cselect_b64 s[12:13], s[8:9], s[12:13]\n"
    "s_mov_b64 s[8:9], s[62:63]\n"
    "s_add_u32 m0, s67, 16384\n"
    "s_nop 0\n"
    "buffer_load_dwordx4 v244, s[8:11], s71 offen offset:0 lds\n"
    "buffer_load_dwordx4 v245, s[8:11], s71 offen offset:1024 lds\n"
    "buffer_load_dwordx4 v246, s[8:11], s71 offen offset:2048 lds\n"
    "s_xor_b32 s78, s78, 2048\n"
    "s_add_u32 m0, s66, s78\n"
    "s_xor_b32 s70, s70, 16384\n"
    "buffer_load_dwordx4 v240, s[4:7], s70 offen offset:0 lds\n"
    "buffer_load_dwordx4 v241, s[4:7], s70 offen offset:1024 lds\n"
    "s_add_u32 m0, s68, 28672\n"
    "s_nop 0\n"
    "buffer_load_dwordx4 v244, s[12:15], s71 offen offset:0 lds\n"
    "buffer_load_dwordx4 v245, s[12:15], s71 offen offset:1024 lds\n"
    "buffer_load_dwordx4 v246, s[12:15], s71 offen offset:2048 lds\n"
    "s_mov_b32 s36, 9216\n"
    "s_lshl_b32 s72, s69, 13\n"
    "s_add_u32 s73, s66, 147456\n"
    "s_and_b32 s74, s69, 1\n"
    "s_lshr_b32 s75, s69, 1\n"
    "s_mul_i32 s76, s69, 11\n"
    "s_add_u32 s77, s76, 10\n"
    "s_cmp_eq_u32 s69, 3\n"
    "s_cselect_b32 s77, 0x8000, s77\n"
    "v_and_b32 v31, 1, v11\n"
    "v_lshlrev_b32 v32, 1, v31\n"
    "v_xor_b32 v33, v12, v32\n"
    "v_lshlrev_b32_e64 v33, 4, v33\n"
    "v_lshl_or_b32 v33, v10, 10, v33\n"
    "s_lshl_b32 s56, s55, 7\n"
    "v_add_u32 v214, s56, v33\n"
    "v_xor_b32_e64 v215, 64, v214\n"
    "v_add_u32 v215, 7168, v215\n"
    "v_add_u32 v216, 14336, v214\n"
    "v_add_u32 v217, 14336, v215\n"
    "v_bfe_u32 v35, v255, 2, 2\n"
    "v_lshl_or_b32 v36, v11, 3, v35\n"
    "v_and_b32 v30, 3, v255\n"
    "v_lshlrev_b32 v37, 3, v30\n"
    "v_lshl_or_b32 v36, v36, 7, v37\n"
    "v_and_b32 v37, 3, v10\n"
    "s_lshl_b32 s56, s75, 1\n"
    "v_xor_b32_e64 v37, s56, v37\n"
    "v_lshl_or_b32 v36, v37, 5, v36\n"
    "v_xor_b32 v37, 0, v36\n"
    "v_add_u32 v200, s72, v37\n"
    "v_add_u32 v204, 81920, v200\n"
    "v_add_u32 v208, s73, v37\n"
    "v_xor_b32 v37, 32, v36\n"
    "v_add_u32_e64 v201, s72, v37\n"
    "v_add_u32 v205, 81920, v201\n"
    "v_add_u32 v209, s73, v37\n"
    "v_xor_b32 v37, 64, v36\n"
    "v_add_u32_e64 v202, s72, v37\n"
    "v_add_u32 v206, 81920, v202\n"
    "v_add_u32_e64 v210, s73, v37\n"
    "v_xor_b32 v37, 96, v36\n"
    "v_add_u32_e64 v203, s72, v37\n"
    "v_add_u32 v207, 81920, v203\n"
    "v_add_u32 v211, s73, v37\n"
    "v_and_b32 v38, 15, v255\n"
    "v_lshlrev_b32_e64 v36, 4, v38\n"
    "v_lshl_or_b32 v37, v11, 9, v36\n"
    "s_lshl1_add_u32 s56, s54, s74\n"
    "s_xor_b32 s58, s56, 1\n"
    "s_mul_i32 s60, s56, 88064\n"
    "s_mul_i32 s58, s58, 88064\n"
    "s_add_u32 s60, s60, 4718592\n"
    "s_add_u32 s58, s58, 4718592\n"
    "v_add_u32 v213, s60, v37\n"
    "v_add_u32 v212, s58, v37\n"
    "s_lshl_b32 s58, s56, 8\n"
    "s_add_u32 s58, s58, 4096\n"
    "v_add_u32 v249, s58, v36\n"
    "s_lshl1_add_u32 s58, s55, s75\n"
    "s_mul_i32 s58, s58, 135168\n"
    "s_lshl_b32 s56, s56, 6\n"
    "s_add_u32 s58, s58, s56\n"
    "s_add_u32 s79, s58, 2490368\n"
    "s_add_u32 s80, s58, 2557952\n"
    "v_mul_u32_u24 v36, 4224, v38\n"
    "v_and_or_b32 v253, v255, 48, v36\n"
    "s_lshl_b32 s56, s69, 3\n"
    "v_min_u32 v36, 9, v255\n"
    "v_add_u32 v36, s56, v36\n"
    "v_min_u32 v36, 31, v36\n"
    "s_lshl_b32 s56, s54, 12\n"
    "s_add_u32 s56, s56, 131072\n"
    "v_lshl_add_u32 v248, v36, 7, s56\n"
    "s_lshr_b32 s58, s2, 4\n"
    "s_lshl2_add_u32 s58, s58, s69\n"
    "s_lshl_b32 s58, s58, 11\n"
    "s_add_u32 s58, s58, 10616832\n"
    "v_lshl_add_u32 v36, v38, 7, s58\n"
    "s_lshr_b32 s58, s2, 3\n"
    "s_lshl2_add_u32 s58, s58, s69\n"
    "s_lshl_b32 s58, s58, 10\n"
    "s_add_u32 s58, s58, 10747904\n"
    "v_cmp_gt_u32 vcc, 16, v255\n"
    "v_lshl_add_u32 v37, v12, 7, s58\n"
    "s_mul_i32 s58, s2, 8448\n"
    "v_cndmask_b32 v250, v37, v36, vcc\n"
    "s_lshl_b32 s56, s69, 11\n"
    "s_add_u32 s40, s56, 131840\n"
    "s_add_u32 s58, s58, s56\n"
    "s_add_u32 s58, s58, 262144\n"
    "v_add_u32_e64 v251, s58, v254\n"
    "v_lshl_or_b32 v37, v11, 3, v35\n"
    "v_lshrrev_b32_e64 v39, 1, v35\n"
    "v_lshl_or_b32 v39, v31, 2, v39\n"
    "v_lshrrev_b32 v36, 1, v30\n"
    "v_xor_b32 v36, v36, v39\n"
    "s_lshr_b32 s58, s50, 3\n"
    "s_lshl_b32 s56, s58, 2\n"
    "v_xor_b32 v36, s56, v36\n"
    "v_and_b32 v39, 1, v30\n"
    "v_lshl_or_b32 v36, v36, 1, v39\n"
    "v_lshlrev_b32_e64 v36, 3, v36\n"
    "v_lshl_or_b32 v36, v37, 7, v36\n"
    "v_add_u32 v80, s66, v36\n"
    "v_xor_b32 v81, 32, v80\n"
    "s_lshl1_add_u32 s61, s52, s58\n"
    "s_lshl2_add_u32 s58, s49, s69\n"
    "s_mul_i32 s56, s61, 135168\n"
    "s_lshl_b32 s60, s58, 7\n"
    "s_add_u32 s56, s56, s60\n"
    "s_add_u32 s42, s56, 262144\n"
    "s_add_u32 s43, s56, 329728\n"
    "v_lshrrev_b32_e64 v36, 1, v38\n"
    "v_mul_u32_u24 v36, 8448, v36\n"
    "v_lshl_or_b32 v39, v11, 1, v39\n"
    "v_lshl_add_u32 v62, v39, 4, v36\n"
    "s_lshl_b32 s56, s61, 13\n"
    "s_add_u32 s56, s56, s60\n"
    "s_add_u32 s56, s56, 10616832\n"
    "v_mov_b32 v63, s56\n"
    "s_lshl2_add_u32 s61, s55, s69\n"
    "s_lshl_b32 s56, s61, 12\n"
    "s_lshl_b32 s60, s54, 7\n"
    "s_add_u32 s56, s56, s60\n"
    "s_add_u32 s56, s56, 10747904\n"
    "v_writelane_b32 v63, s56, 1\n"
    "s_mul_i32 s56, s61, 67584\n"
    "s_add_u32 s56, s56, s60\n"
    "s_add_u32 s44, s56, 2490368\n"
    "s_add_u32 s45, s56, 2524160\n"
    "v_lshrrev_b32 v40, 5, v255\n"
    "v_and_b32 v41, 31, v255\n"
    "s_add_u32 s56, s69, 1\n"
    "s_lshl_b32 s58, s56, 9\n"
    "s_lshl_b32 s60, 64, s69\n"
    "s_cmp_eq_u32 s69, 0\n"
    "s_cselect_b32 s58, 0, s58\n"
    "s_cselect_b32 s60, 0, s60\n"
    "s_cselect_b32 s56, 2, s56\n"
    "s_cselect_b32 s61, 5, 4\n"
    "v_lshlrev_b32_e64 v42, s61, v41\n"
    "v_mad_u32_u24 v43, v40, s36, v42\n"
    "s_add_u32 s58, s58, 145408\n"
    "v_add_u32 v60, s58, v43\n"
    "v_lshlrev_b32 v42, s56, v41\n"
    "v_mul_u32_u24 v43, 4352, v40\n"
    "s_add_u32 s60, s60, 122944\n"
    "v_add3_u32 v61, v42, v43, s60\n"
    "v_lshlrev_b32_e64 v59, 4, v41\n"
    "v_lshl_or_b32 v58, v40, 10, v59\n"
    "s_lshl4_add_u32 s56, s49, s50\n"
    "s_lshl2_add_u32 s56, s56, s69\n"
    "s_lshl_b32 s36, s56, 11\n"
    "s_lshr_b32 s60, s56, 4\n"
    "s_lshl_b32 s60, s60, 8\n"
    "s_and_b32 s58, s56, 7\n"
    "s_lshl_b32 s58, s58, 5\n"
    "s_add_u32 s60, s60, s58\n"
    "s_and_b32 s58, s56, 8\n"
    "s_add_u32 s60, s60, s58\n"
    "s_add_u32 s37, s60, 4096\n"
    "s_waitcnt lgkmcnt(0)\n"
    "s_add_u32 s46, s46, 1\n"
    "s_addc_u32 s47, s47, 0\n"
    "v_mov_b32 v84, s46\n"
    "v_mov_b32 v85, s47\n"
    "v_mov_b32 v40, -1\n"
    "v_mov_b32 v41, -1\n"
    "v_mov_b32 v42, -1\n"
    "v_mov_b32 v43, -1\n"
    "v_add_u32 v232, s72, v254\n"
    "s_lshl_b32 s56, s75, 12\n"
    "s_xor_b32 s58, s56, 4096\n"
    "v_add_u32 v230, s56, v232\n"
    "v_add_u32 v231, s58, v232\n"
    "s_xor_b32 s58, s74, 1\n"
    "s_lshl_b32 s58, s58, 13\n"
    "s_add_u32 s58, s58, s56\n"
    "v_add_u32_e64 v233, s58, v254\n"
    "v_xor_b32 v234, 16384, v232\n"
    "v_and_b32 v235, 16, v254\n"
    "v_lshrrev_b32 v36, 1, v38\n"
    "v_mad_i32_i24 v237, v11, -4, v36\n"
    "v_and_b32 v36, 31, v255\n"
    "v_lshlrev_b32 v36, 10, v36\n"
    "v_lshrrev_b32_e64 v37, 5, v255\n"
    "v_lshl_or_b32 v236, v37, 4, v36\n"
    "s_lshl1_add_u32 s56, s54, s74\n"
    "s_lshl_b32 s56, s56, 15\n"
    "s_lshl1_add_u32 s58, s55, s75\n"
    "s_lshl_b32 s58, s58, 6\n"
    "s_add_u32 s41, s56, s58\n"
    "v_mul_u32_u24 v36, 4224, v10\n"
    "v_lshl_add_u32 v86, v12, 4, v36\n"
    "s_lshl2_add_u32 s56, s2, s69\n"
    "s_mul_i32 s56, s56, 11\n"
    "s_min_u32 s56, s56, 10997\n"
    "s_lshl_b32 s56, s56, 7\n"
    "s_add_u32 s38, s16, s56\n"
    "s_addc_u32 s39, s17, 0\n"
    "s_lshr_b32 s56, s2, 3\n"
    "s_lshl2_add_u32 s56, s56, s69\n"
    "s_mul_i32 s56, s56, 11\n"
    "s_min_u32 s56, s56, 1365\n"
    "s_lshl_b32 s56, s56, 10\n"
    "s_sub_u32 s58, 384, s16\n"
    "s_and_b32 s58, s58, 0x380\n"
    "s_add_u32 s56, s56, s58\n"
    "s_add_u32 s34, s16, s56\n"
    "s_addc_u32 s35, s17, 0\n"
    "buffer_load_dwordx4 v[64:67], v58, s[4:7], s36 offen\n"
    "buffer_load_dwordx4 v[68:71], v58, s[4:7], s36 offen offset:512\n"
    "global_load_dwordx4 v[72:75], v59, s[28:29]\n"
    "global_load_dwordx4 v[76:79], v59, s[28:29] offset:512\n"
    "s_add_u32 s51, s51, 1\n"
    "s_and_b32 s51, s51, 7\n"
    "s_mul_i32 s71, s51, 176128\n"
    "s_add_u32 s71, s71, s65\n"
    "s_add_u32 m0, s67, 57344\n"
    "s_nop 0\n"
    "buffer_load_dwordx4 v244, s[8:11], s71 offen offset:0 lds\n"
    "buffer_load_dwordx4 v245, s[8:11], s71 offen offset:1024 lds\n"
    "buffer_load_dwordx4 v246, s[8:11], s71 offen offset:2048 lds\n"
    "s_add_u32 m0, s66, 40960\n"
    "s_lshl_b32 s70, s51, 7\n"
    "s_add_u32 s70, s70, s64\n"
    "buffer_load_dwordx4 v240, s[4:7], s70 offen offset:0 lds\n"
    "buffer_load_dwordx4 v241, s[4:7], s70 offen offset:1024 lds\n"
    "buffer_load_dwordx4 v242, s[4:7], s70 offen offset:2048 lds\n"
    "buffer_load_dwordx4 v243, s[4:7], s70 offen offset:3072 lds\n"
    "s_add_u32 m0, s68, 69632\n"
    "s_nop 0\n"
    "buffer_load_dwordx4 v244, s[12:15], s71 offen offset:0 lds\n"
    "buffer_load_dwordx4 v245, s[12:15], s71 offen offset:1024 lds\n"
    "buffer_load_dwordx4 v246, s[12:15], s71 offen offset:2048 lds\n"
    "s_waitcnt vmcnt(17)\n"
    "ds_read_b64_tr_b16 v[48:49], v80 offset:0\n"
    "ds_read_b64_tr_b16 v[50:51], v81 offset:512\n"
    "ds_read_b64_tr_b16 v[52:53], v81 offset:0\n"
    "ds_read_b64_tr_b16 v[54:55], v80 offset:512\n"
    "buffer_store_dwordx4 v[40:43], v86, s[20:23], s44 offen sc1\n"
    "buffer_store_dwordx4 v[40:43], v86, s[20:23], s45 offen sc1\n"
    "s_waitcnt lgkmcnt(2)\n"
    "buffer_store_dwordx4 v[48:51], v62, s[20:23], s42 offen sc1\n"
    "s_waitcnt lgkmcnt(0)\n"
    "buffer_store_dwordx4 v[52:55], v62, s[20:23], s43 offen sc1\n"
    "v_mov_b32 v32, 0\n"
    "v_mov_b32 v33, 0\n"
    "s_waitcnt vmcnt(15)\n"
    "v_dot2c_f32_bf16 v32, v64, v72\n"
    "v_dot2c_f32_bf16 v33, v65, v73\n"
    "v_dot2c_f32_bf16 v32, v66, v74\n"
    "v_dot2c_f32_bf16 v33, v67, v75\n"
    "s_waitcnt vmcnt(14)\n"
    "v_dot2c_f32_bf16 v32, v68, v76\n"
    "v_dot2c_f32_bf16 v33, v69, v77\n"
    "v_dot2c_f32_bf16 v32, v70, v78\n"
    "v_dot2c_f32_bf16 v33, v71, v79\n"
    "v_lshrrev_b32 v34, 5, v255\n"
    "v_lshlrev_b32_e64 v34, 4, v34\n"
    "v_mov_b32 v35, 0xffff\n"
    "v_add_f32 v32, v32, v33\n"
    "s_nop 1\n"
    "v_add_f32_dpp v32, v32, v32 row_shr:1 row_mask:0xf bank_mask:0xf bound_ctrl:1\n"
    "s_nop 1\n"
    "v_add_f32_dpp v32, v32, v32 row_shr:2 row_mask:0xf bank_mask:0xf bound_ctrl:1\n"
    "s_nop 1\n"
    "v_add_f32_dpp v32, v32, v32 row_shr:4 row_mask:0xf bank_mask:0xf bound_ctrl:1\n"
    "s_nop 1\n"
    "v_add_f32_dpp v32, v32, v32 row_shr:8 row_mask:0xf bank_mask:0xf bound_ctrl:1\n"
    "s_nop 1\n"
    "v_add_f32_dpp v32, v32, v32 row_bcast:15 row_mask:0xa bank_mask:0xf\n"
    "v_cvt_pk_bf16_f32 v32, v32, v32\n"
    "v_and_b32 v32, 0xffff0000, v32\n"
    "v_mul_f32 v32, 0xbfb8aa3b, v32\n"
    "v_exp_f32 v32, v32\n"
    "v_mov_b32 v33, s46\n"
    "v_add_f32 v32, 1.0, v32\n"
    "v_rcp_f32_e64 v32, v32\n"
    "s_nop 0\n"
    "v_cvt_pk_bf16_f32 v32, v32, v32\n"
    "v_bfi_b32 v32, v35, s47, v32\n"
    "s_mov_b32 exec_lo, 0x80000000\n"
    "s_mov_b32 exec_hi, 0x80000000\n"
    "buffer_store_dwordx2 v[32:33], v34, s[20:23], s37 offen sc1\n"
    "s_mov_b64 exec, -1\n"
    "s_waitcnt vmcnt(19)\n"
    "s_barrier\n"
    "ds_read_b128 v[16:19], v60 offset:0\n"
    "ds_read_b128 v[20:23], v60 offset:16\n"
    "ds_read_b128 v[24:27], v60 offset:4608\n"
    "ds_read_b128 v[28:31], v60 offset:4624\n"
    "s_add_u32 s51, s51, 1\n"
    "s_and_b32 s51, s51, 7\n"
    "s_mul_i32 s71, s51, 176128\n"
    "s_add_u32 s71, s71, s65\n"
    "s_add_u32 m0, s67, 98304\n"
    "s_nop 0\n"
    "buffer_load_dwordx4 v244, s[8:11], s71 offen offset:0 lds\n"
    "buffer_load_dwordx4 v245, s[8:11], s71 offen offset:1024 lds\n"
    "buffer_load_dwordx4 v246, s[8:11], s71 offen offset:2048 lds\n"
    "s_add_u32 m0, s66, 81920\n"
    "s_lshl_b32 s70, s51, 7\n"
    "s_add_u32 s70, s70, s64\n"
    "buffer_load_dwordx4 v240, s[4:7], s70 offen offset:0 lds\n"
    "buffer_load_dwordx4 v241, s[4:7], s70 offen offset:1024 lds\n"
    "buffer_load_dwordx4 v242, s[4:7], s70 offen offset:2048 lds\n"
    "buffer_load_dwordx4 v243, s[4:7], s70 offen offset:3072 lds\n"
    "s_add_u32 m0, s68, 110592\n"
    "s_nop 0\n"
    "buffer_load_dwordx4 v244, s[12:15], s71 offen offset:0 lds\n"
    "buffer_load_dwordx4 v245, s[12:15], s71 offen offset:1024 lds\n"
    "buffer_load_dwordx4 v246, s[12:15], s71 offen offset:2048 lds\n"
    "s_waitcnt lgkmcnt(1)\n"
    "v_add_f32 v16, v16, v24\n"
    "v_add_f32 v17, v17, v25\n"
    "v_add_f32 v18, v18, v26\n"
    "v_add_f32 v19, v19, v27\n"
    "s_waitcnt lgkmcnt(0)\n"
    "v_add_f32 v20, v20, v28\n"
    "v_add_f32 v21, v21, v29\n"
    "v_add_f32 v22, v22, v30\n"
    "v_add_f32 v23, v23, v31\n"
    "s_cmp_lg_u32 s69, 3\n"
    "s_cbranch_scc1 .Lx7_%=\n"
    "v_cvt_pk_bf16_f32 v24, v16, v16\n"
    "v_cvt_pk_bf16_f32 v25, v17, v18\n"
    "v_cvt_pk_bf16_f32 v26, v18, v18\n"
    "v_cvt_pk_bf16_f32 v27, v19, v20\n"
    "v_cvt_pk_bf16_f32 v28, v16, v17\n"
    "v_cvt_pk_bf16_f32 v29, v17, v18\n"
    "v_cvt_pk_bf16_f32 v30, v18, v19\n"
    "v_cvt_pk_bf16_f32 v31, v19, v20\n"
    "s_branch .Lx6_%=\n"
    ".Lx7_%=:\n"
    "s_cmp_lg_u32 s69, 2\n"
    "s_cbranch_scc1 .Lx8_%=\n"
    "s_nop 0\n"
    "v_cvt_pk_bf16_f32 v24, v16, v17\n"
    "v_cvt_pk_bf16_f32 v25, v18, v19\n"
    "v_cvt_pk_bf16_f32 v26, v16, v18\n"
    "v_cvt_pk_bf16_f32 v27, v18, v20\n"
    "v_cvt_pk_bf16_f32 v28, v17, v18\n"
    "v_cvt_pk_bf16_f32 v29, v19, v20\n"
    "s_branch .Lx6_%=\n"
    ".Lx8_%=:\n"
    "s_cmp_lg_u32 s69, 1\n"
    "s_cbranch_scc1 .Lx9_%=\n"
    "v_cvt_pk_bf16_f32 v24, v16, v18\n"
    "v_cvt_pk_bf16_f32 v25, v17, v19\n"
    "v_cvt_pk_bf16_f32 v26, v18, v20\n"
    "s_branch .Lx6_%=\n"
    ".Lx9_%=:\n"
    "v_cvt_pk_bf16_f32 v24, v16, v20\n"
    "v_cvt_pk_bf16_f32 v25, v17, v21\n"
    "v_cvt_pk_bf16_f32 v26, v18, v22\n"
    "v_cvt_pk_bf16_f32 v27, v19, v23\n"
    ".Lx6_%=:\n"
    "s_load_dword s81, s[38:39], 0\n"
    "s_load_dword s81, s[38:39], 64\n"
    "s_load_dword s81, s[38:39], 128\n"
    "s_load_dword s81, s[38:39], 192\n"
    "s_load_dword s81, s[38:39], 256\n"
    "s_load_dword s81, s[38:39], 320\n"
    "s_waitcnt vmcnt(15)\n"
    "s_barrier\n"
    "s_add_u32 s51, s51, 1\n"
    "s_and_b32 s51, s51, 7\n"
    "s_mul_i32 s71, s51, 176128\n"
    "s_add_u32 s71, s71, s65\n"
    "s_add_u32 m0, s67, 16384\n"
    "s_nop 0\n"
    "buffer_load_dwordx4 v244, s[8:11], s71 offen offset:0 lds\n"
    "buffer_load_dwordx4 v245, s[8:11], s71 offen offset:1024 lds\n"
    "buffer_load_dwordx4 v246, s[8:11], s71 offen offset:2048 lds\n"
    "s_add_u32 m0, s66, 0\n"
    "s_lshl_b32 s70, s51, 7\n"
    "s_add_u32 s70, s70, s64\n"
    "buffer_load_dwordx4 v240, s[4:7], s70 offen offset:0 lds\n"
    "s_waitcnt vmcnt(15)\n"
    "s_mov_b64 exec, 3\n"
    "buffer_store_dwordx2 v[84:85], v63, s[20:23], 0 offen sc1\n"
    "s_mov_b64 exec, -1\n"
    "buffer_load_dwordx4 v241, s[4:7], s70 offen offset:1024 lds\n"
    "buffer_load_dwordx4 v242, s[4:7], s70 offen offset:2048 lds\n"
    "buffer_load_dwordx4 v243, s[4:7], s70 offen offset:3072 lds\n"
    "s_add_u32 m0, s68, 28672\n"
    "s_nop 0\n"
    "buffer_load_dwordx4 v244, s[12:15], s71 offen offset:0 lds\n"
    "buffer_load_dwordx4 v245, s[12:15], s71 offen offset:1024 lds\n"
    "buffer_load_dwordx4 v246, s[12:15], s71 offen offset:2048 lds\n"
    "s_cmp_lg_u32 s69, 3\n"
    "s_cbranch_scc1 .Lx11_%=\n"
    "ds_write_b128 v61, v[24:27] offset:0\n"
    "ds_write_b128 v61, v[28:31] offset:1088\n"
    "s_branch .Lx10_%=\n"
    ".Lx11_%=:\n"
    "s_cmp_lg_u32 s69, 2\n"
    "s_cbranch_scc1 .Lx12_%=\n"
    "ds_write_b64 v61, v[24:25] offset:0\n"
    "ds_write_b64 v61, v[24:25] offset:1088\n"
    "s_branch .Lx10_%=\n"
    ".Lx12_%=:\n"
    "s_cmp_lg_u32 s69, 1\n"
    "s_cbranch_scc1 .Lx13_%=\n"
    "ds_write_b32 v61, v24 offset:0\n"
    "ds_write_b32 v61, v24 offset:1088\n"
    "s_branch .Lx10_%=\n"
    ".Lx13_%=:\n"
    "ds_write_b32 v61, v24 offset:0\n"
    "ds_write_b32 v61, v25 offset:1088\n"
    ".Lx10_%=:\n"
    "s_load_dword s81, s[38:39], 384\n"
    "s_load_dword s81, s[38:39], 448\n"
    "s_load_dword s81, s[38:39], 512\n"
    "s_load_dword s81, s[38:39], 576\n"
    "s_load_dword s81, s[38:39], 640\n"
    "s_load_dword s81, s[38:39], 704\n"
    "s_waitcnt vmcnt(11)\n"
    "s_barrier\n"
    "s_add_u32 s51, s51, 1\n"
    "s_and_b32 s51, s51, 7\n"
    "s_mul_i32 s71, s51, 176128\n"
    "s_add_u32 s71, s71, s65\n"
    "s_add_u32 m0, s67, 57344\n"
    "s_nop 0\n"
    "buffer_load_dwordx4 v244, s[8:11], s71 offen offset:0 lds\n"
    "buffer_load_dwordx4 v245, s[8:11], s71 offen offset:1024 lds\n"
    "buffer_load_dwordx4 v246, s[8:11], s71 offen offset:2048 lds\n"
    "s_add_u32 m0, s66, 40960\n"
    "s_lshl_b32 s70, s51, 7\n"
    "s_add_u32 s70, s70, s64\n"
    "buffer_load_dwordx4 v240, s[4:7], s70 offen offset:0 lds\n"
    "buffer_load_dwordx4 v241, s[4:7], s70 offen offset:1024 lds\n"
    "buffer_load_dwordx4 v242, s[4:7], s70 offen offset:2048 lds\n"
    "buffer_load_dwordx4 v243, s[4:7], s70 offen offset:3072 lds\n"
    "s_add_u32 m0, s68, 69632\n"
    "s_nop 0\n"
    "buffer_load_dwordx4 v244, s[12:15], s71 offen offset:0 lds\n"
    "buffer_load_dwordx4 v245, s[12:15], s71 offen offset:1024 lds\n"
    "buffer_load_dwordx4 v246, s[12:15], s71 offen offset:2048 lds\n"
    "s_cmp_lg_u32 s69, 3\n"
    "s_cbranch_scc1 .Lx15_%=\n"
    "ds_write_b128 v61, v[28:31] offset:2176\n"
    "ds_write_b128 v61, v[28:31] offset:3264\n"
    "s_branch .Lx14_%=\n"
    ".Lx15_%=:\n"
    "s_cmp_lg_u32 s69, 2\n"
    "s_cbranch_scc1 .Lx16_%=\n"
    "ds_write_b64 v61, v[26:27] offset:2176\n"
    "ds_write_b64 v61, v[28:29] offset:3264\n"
    "s_branch .Lx14_%=\n"
    ".Lx16_%=:\n"
    "s_cmp_lg_u32 s69, 1\n"
    "s_cbranch_scc1 .Lx17_%=\n"
    "ds_write_b32 v61, v25 offset:2176\n"
    "ds_write_b32 v61, v26 offset:3264\n"
    "s_branch .Lx14_%=\n"
    ".Lx17_%=:\n"
    "ds_write_b32 v61, v26 offset:2176\n"
    "ds_write_b32 v61, v27 offset:3264\n"
    ".Lx14_%=:\n"
    "s_load_dword s81, s[38:39], 768\n"
    "s_load_dword s81, s[38:39], 832\n"
    "s_load_dword s81, s[38:39], 896\n"
    "s_load_dword s81, s[38:39], 960\n"
    "s_load_dword s81, s[38:39], 1024\n"
    "s_load_dword s81, s[38:39], 1088\n"
    "s_load_dword s81, s[38:39], 1152\n"
    "s_waitcnt vmcnt(10)\n"
    "s_barrier\n"
    "s_add_u32 s51, s51, 1\n"
    "s_and_b32 s51, s51, 7\n"
    "s_mul_i32 s71, s51, 176128\n"
    "s_add_u32 s71, s71, s65\n"
    "s_add_u32 m0, s67, 98304\n"
    "s_nop 0\n"
    "buffer_load_dwordx4 v244, s[8:11], s71 offen offset:0 lds\n"
    "buffer_load_dwordx4 v245, s[8:11], s71 offen offset:1024 lds\n"
    "buffer_load_dwordx4 v246, s[8:11], s71 offen offset:2048 lds\n"
    "s_add_u32 m0, s66, 81920\n"
    "s_lshl_b32 s70, s51, 7\n"
    "s_add_u32 s70, s70, s64\n"
    "buffer_load_dwordx4 v240, s[4:7], s70 offen offset:0 lds\n"
    "buffer_load_dwordx4 v241, s[4:7], s70 offen offset:1024 lds\n"
    "buffer_load_dwordx4 v242, s[4:7], s70 offen offset:2048 lds\n"
    "buffer_load_dwordx4 v243, s[4:7], s70 offen offset:3072 lds\n"
    "s_add_u32 m0, s68, 110592\n"
    "s_nop 0\n"
    "buffer_load_dwordx4 v244, s[12:15], s71 offen offset:0 lds\n"
    "buffer_load_dwordx4 v245, s[12:15], s71 offen offset:1024 lds\n"
    "buffer_load_dwordx4 v246, s[12:15], s71 offen offset:2048 lds\n"
    "s_load_dword s81, s[38:39], 1216\n"
    "s_load_dword s81, s[38:39], 1280\n"
    "s_load_dword s81, s[38:39], 1344\n"
    "s_load_dword s81, s[34:35], 0\n"
    "s_load_dword s81, s[34:35], 64\n"
    "s_load_dword s81, s[34:35], 1024\n"
    "s_load_dword s81, s[34:35], 1088\n"
    "s_load_dword s81, s[34:35], 2048\n"
    "s_load_dword s81, s[34:35], 2112\n"
    "s_waitcnt vmcnt(10)\n"
    "s_barrier\n"
    "buffer_load_dwordx2 v[56:57], v250, s[20:23], 0 offen sc1\n"
    "s_add_u32 s51, s51, 1\n"
    "s_and_b32 s51, s51, 7\n"
    "s_mul_i32 s71, s51, 176128\n"
    "s_add_u32 s71, s71, s65\n"
    "s_add_u32 m0, s67, 16384\n"
    "s_nop 0\n"
    "buffer_load_dwordx4 v244, s[8:11], s71 offen offset:0 lds\n"
    "buffer_load_dwordx4 v245, s[8:11], s71 offen offset:1024 lds\n"
    "buffer_load_dwordx4 v246, s[8:11], s71 offen offset:2048 lds\n"
    "s_add_u32 m0, s66, 0\n"
    "s_lshl_b32 s70, s51, 7\n"
    "s_add_u32 s70, s70, s64\n"
    "buffer_load_dwordx4 v240, s[4:7], s70 offen offset:0 lds\n"
    "buffer_load_dwordx4 v241, s[4:7], s70 offen offset:1024 lds\n"
    "buffer_load_dwordx4 v242, s[4:7], s70 offen offset:2048 lds\n"
    "buffer_load_dwordx4 v243, s[4:7], s70 offen offset:3072 lds\n"
    "s_add_u32 m0, s68, 28672\n"
    "s_nop 0\n"
    "buffer_load_dwordx4 v244, s[12:15], s71 offen offset:0 lds\n"
    "buffer_load_dwordx4 v245, s[12:15], s71 offen offset:1024 lds\n"
    "buffer_load_dwordx4 v246, s[12:15], s71 offen offset:2048 lds\n"
    "s_load_dword s81, s[34:35], 3072\n"
    "s_load_dword s81, s[34:35], 3136\n"
    "s_load_dword s81, s[34:35], 4096\n"
    "s_load_dword s81, s[34:35], 4160\n"
    "s_load_dword s81, s[34:35], 5120\n"
    "s_load_dword s81, s[34:35], 5184\n"
    "s_load_dword s81, s[34:35], 6144\n"
    "s_load_dword s81, s[34:35], 6208\n"
    "s_load_dword s81, s[34:35], 7168\n"
    "s_waitcnt vmcnt(11)\n"
    "s_barrier\n"
    "s_waitcnt vmcnt(10)\n"
    "v_cmp_ne_u64 vcc, s[46:47], v[56:57]\n"
    "s_and_b64 vcc, vcc, 0xffffff\n"
    "s_cbranch_vccz .Lspd19_%=\n"
    "s_mov_b32 s63, 0x1000000\n"
    ".Lsp18_%=:\n"
    "s_sleep 2\n"
    "v_mov_b32 v56, 0\n"
    "v_mov_b32 v57, 0\n"
    "s_mov_b64 exec, 0xffffff\n"
    "buffer_atomic_or_x2 v[56:57], v250, s[20:23], 0 offen sc0 sc1\n"
    "s_mov_b64 exec, -1\n"
    "s_waitcnt vmcnt(0)\n"
    "v_cmp_ne_u64 vcc, s[46:47], v[56:57]\n"
    "s_and_b64 vcc, vcc, 0xffffff\n"
    "s_cbranch_vccz .Lspd19_%=\n"
    "s_sub_u32 s63, s63, 1\n"
    "s_cbranch_scc0 .Lsp18_%=\n"
    ".Lspd19_%=:\n"
    "s_mov_b32 m0, s40\n"
    "s_nop 0\n"
    "buffer_load_dwordx4 v251, s[20:23], 0 offen offset:0 sc1 lds\n"
    "buffer_load_dwordx4 v251, s[20:23], 0 offen offset:1024 sc1 lds\n"
    "s_add_u32 s51, s51, 1\n"
    "s_and_b32 s51, s51, 7\n"
    "s_mul_i32 s71, s51, 176128\n"
    "s_add_u32 s71, s71, s65\n"
    "s_add_u32 m0, s67, 57344\n"
    "s_nop 0\n"
    "buffer_load_dwordx4 v244, s[8:11], s71 offen offset:0 lds\n"
    "buffer_load_dwordx4 v245, s[8:11], s71 offen offset:1024 lds\n"
    "buffer_load_dwordx4 v246, s[8:11], s71 offen offset:2048 lds\n"
    "s_add_u32 m0, s66, 40960\n"
    "s_lshl_b32 s70, s51, 7\n"
    "s_add_u32 s70, s70, s64\n"
    "buffer_load_dwordx4 v240, s[4:7], s70 offen offset:0 lds\n"
    "buffer_load_dwordx4 v241, s[4:7], s70 offen offset:1024 lds\n"
    "buffer_load_dwordx4 v242, s[4:7], s70 offen offset:2048 lds\n"
    "buffer_load_dwordx4 v243, s[4:7], s70 offen offset:3072 lds\n"
    "s_add_u32 m0, s68, 69632\n"
    "s_nop 0\n"
    "buffer_load_dwordx4 v244, s[12:15], s71 offen offset:0 lds\n"
    "buffer_load_dwordx4 v245, s[12:15], s71 offen offset:1024 lds\n"
    "buffer_load_dwordx4 v246, s[12:15], s71 offen offset:2048 lds\n"
    "s_load_dword s81, s[34:35], 7232\n"
    "s_load_dword s81, s[34:35], 8192\n"
    "s_load_dword s81, s[34:35], 8256\n"
    "s_load_dword s81, s[34:35], 9216\n"
    "s_load_dword s81, s[34:35], 9280\n"
    "s_load_dword s81, s[34:35], 10240\n"
    "s_load_dword s81, s[34:35], 10304\n"
    "s_waitcnt vmcnt(12)\n"
    "s_barrier\n"
    "s_add_u32 m0, s73, 0\n"
    "s_add_u32 s71, s76, 0\n"
    "s_lshl_b32 s71, s71, 15\n"
    "buffer_load_dwordx4 v214, s[16:19], s71 offen offset:0 lds\n"
    "buffer_load_dwordx4 v215, s[16:19], s71 offen offset:1024 lds\n"
    "buffer_load_dwordx4 v216, s[16:19], s71 offen offset:2048 lds\n"
    "buffer_load_dwordx4 v217, s[16:19], s71 offen offset:3072 lds\n"
    "s_add_u32 m0, s72, 81920\n"
    "s_add_u32 s71, s76, 1\n"
    "s_lshl_b32 s71, s71, 15\n"
    "buffer_load_dwordx4 v214, s[16:19], s71 offen offset:0 lds\n"
    "buffer_load_dwordx4 v215, s[16:19], s71 offen offset:1024 lds\n"
    "buffer_load_dwordx4 v216, s[16:19], s71 offen offset:2048 lds\n"
    "buffer_load_dwordx4 v217, s[16:19], s71 offen offset:3072 lds\n"
    "s_add_u32 m0, s72, 86016\n"
    "s_add_u32 s71, s76, 2\n"
    "s_lshl_b32 s71, s71, 15\n"
    "buffer_load_dwordx4 v214, s[16:19], s71 offen offset:0 lds\n"
    "buffer_load_dwordx4 v215, s[16:19], s71 offen offset:1024 lds\n"
    "s_waitcnt vmcnt(10)\n"
    "s_barrier\n"
    "buffer_load_dwordx4 v216, s[16:19], s71 offen offset:2048 lds\n"
    "buffer_load_dwordx4 v217, s[16:19], s71 offen offset:3072 lds\n"
    "s_add_u32 m0, s72, 0\n"
    "s_add_u32 s71, s76, 3\n"
    "s_lshl_b32 s71, s71, 15\n"
    "buffer_load_dwordx4 v214, s[16:19], s71 offen offset:0 lds\n"
    "buffer_load_dwordx4 v215, s[16:19], s71 offen offset:1024 lds\n"
    "s_waitcnt vmcnt(24) lgkmcnt(0)\n"
    "s_barrier\n"
    "buffer_load_dwordx4 v[196:199], v249, s[20:23], 0 offen sc1\n"
    "s_waitcnt vmcnt(6)\n"
    "buffer_load_dwordx4 v216, s[16:19], s71 offen offset:2048 lds\n"
    "s_add_u32 s28, s76, 0\n"
    "s_lshl_b32 s28, s28, 11\n"
    "s_add_u32 s29, s76, 1\n"
    "s_lshl_b32 s29, s29, 11\n"
    "s_waitcnt vmcnt(6)\n"
    "buffer_load_dwordx4 v217, s[16:19], s71 offen offset:3072 lds\n"
    "s_add_u32 s30, s76, 2\n"
    "s_lshl_b32 s30, s30, 11\n"
    "s_add_u32 s31, s76, 3\n"
    "s_lshl_b32 s31, s31, 11\n"
    "s_waitcnt vmcnt(6)\n"
    "s_add_u32 m0, s72, 4096\n"
    "s_add_u32 s71, s76, 4\n"
    "s_lshl_b32 s71, s71, 15\n"
    "buffer_load_dwordx4 v214, s[16:19], s71 offen offset:0 lds\n"
    "s_add_u32 s32, s76, 4\n"
    "s_lshl_b32 s32, s32, 11\n"
    "s_add_u32 s33, s76, 5\n"
    "s_lshl_b32 s33, s33, 11\n"
    "s_waitcnt vmcnt(6)\n"
    "buffer_load_dwordx4 v215, s[16:19], s71 offen offset:1024 lds\n"
    "s_add_u32 s34, s76, 6\n"
    "s_lshl_b32 s34, s34, 11\n"
    "s_add_u32 s35, s76, 7\n"
    "s_lshl_b32 s35, s35, 11\n"
    "s_waitcnt vmcnt(6)\n"
    "buffer_load_dwordx4 v216, s[16:19], s71 offen offset:2048 lds\n"
    "s_add_u32 s36, s76, 8\n"
    "s_lshl_b32 s36, s36, 11\n"
    "s_add_u32 s37, s76, 9\n"
    "s_lshl_b32 s37, s37, 11\n"
    "s_waitcnt vmcnt(6)\n"
    "buffer_load_dwordx4 v217, s[16:19], s71 offen offset:3072 lds\n"
    "s_lshl_b32 s38, s77, 11\n"
    "s_and_b32 s56, s47, 0xffff\n"
    "s_waitcnt vmcnt(6)\n"
    "s_add_u32 m0, s72, 40960\n"
    "s_add_u32 s71, s76, 5\n"
    "s_lshl_b32 s71, s71, 15\n"
    "buffer_load_dwordx4 v214, s[16:19], s71 offen offset:0 lds\n"
    "s_waitcnt vmcnt(6)\n"
    "buffer_load_dwordx4 v215, s[16:19], s71 offen offset:1024 lds\n"
    "s_waitcnt vmcnt(6)\n"
    "buffer_load_dwordx4 v216, s[16:19], s71 offen offset:2048 lds\n"
    "s_waitcnt vmcnt(6)\n"
    "buffer_load_dwordx4 v217, s[16:19], s71 offen offset:3072 lds\n"
    "s_waitcnt vmcnt(7)\n"
    "ds_read_b64_tr_b16 v[64:65], v208 offset:0\n"
    "ds_read_b64_tr_b16 v[66:67], v208 offset:512\n"
    "ds_read_b64_tr_b16 v[68:69], v209 offset:0\n"
    "ds_read_b64_tr_b16 v[70:71], v209 offset:512\n"
    "ds_read_b64_tr_b16 v[72:73], v210 offset:0\n"
    "ds_read_b64_tr_b16 v[74:75], v210 offset:512\n"
    "ds_read_b64_tr_b16 v[76:77], v211 offset:0\n"
    "ds_read_b64_tr_b16 v[78:79], v211 offset:512\n"
    "s_waitcnt vmcnt(6)\n"
    "s_add_u32 m0, s72, 45056\n"
    "s_add_u32 s71, s76, 6\n"
    "s_lshl_b32 s71, s71, 15\n"
    "buffer_load_dwordx4 v214, s[16:19], s71 offen offset:0 lds\n"
    "s_waitcnt vmcnt(6)\n"
    "buffer_load_dwordx4 v215, s[16:19], s71 offen offset:1024 lds\n"
    "s_waitcnt vmcnt(6)\n"
    "buffer_load_dwordx4 v216, s[16:19], s71 offen offset:2048 lds\n"
    "s_waitcnt vmcnt(6)\n"
    "buffer_load_dwordx4 v217, s[16:19], s71 offen offset:3072 lds\n"
    "s_waitcnt vmcnt(7)\n"
    "ds_read_b64_tr_b16 v[80:81], v204 offset:0\n"
    "ds_read_b64_tr_b16 v[82:83], v204 offset:512\n"
    "ds_read_b64_tr_b16 v[84:85], v205 offset:0\n"
    "ds_read_b64_tr_b16 v[86:87], v205 offset:512\n"
    "ds_read_b64_tr_b16 v[88:89], v206 offset:0\n"
    "ds_read_b64_tr_b16 v[90:91], v206 offset:512\n"
    "ds_read_b64_tr_b16 v[92:93], v207 offset:0\n"
    "ds_read_b64_tr_b16 v[94:95], v207 offset:512\n"
    "s_waitcnt lgkmcnt(0)\n"
    "s_waitcnt vmcnt(6)\n"
    "s_add_u32 m0, s73, 0\n"
    "s_add_u32 s71, s76, 7\n"
    "s_lshl_b32 s71, s71, 15\n"
    "buffer_load_dwordx4 v214, s[16:19], s71 offen offset:0 lds\n"
    "s_waitcnt vmcnt(6)\n"
    "buffer_load_dwordx4 v215, s[16:19], s71 offen offset:1024 lds\n"
    "v_xor_b32 v14, s56, v196\n"
    "v_xor_b32 v15, s56, v198\n"
    "v_or_b32 v14, v14, v15\n"
    "v_xor_b32_e64 v15, s46, v197\n"
    "v_lshl_or_b32 v14, v14, 16, v15\n"
    "v_xor_b32 v15, s46, v199\n"
    "v_or_b32 v14, v14, v15\n"
    "v_cmp_ne_u32 vcc, 0, v14\n"
    "s_cbranch_vccz .Lspd21_%=\n"
    "s_mov_b32 s63, 0x1000000\n"
    ".Lsp20_%=:\n"
    "s_sleep 4\n"
    "buffer_load_dwordx4 v[196:199], v249, s[20:23], 0 offen sc1\n"
    "s_waitcnt vmcnt(0)\n"
    "v_xor_b32 v14, s56, v196\n"
    "v_xor_b32 v15, s56, v198\n"
    "v_or_b32 v14, v14, v15\n"
    "v_xor_b32 v15, s46, v197\n"
    "v_lshl_or_b32 v14, v14, 16, v15\n"
    "v_xor_b32 v15, s46, v199\n"
    "v_or_b32_e64 v14, v14, v15\n"
    "v_cmp_ne_u32 vcc, 0, v14\n"
    "s_cbranch_vccz .Lspd21_%=\n"
    "s_sub_u32 s63, s63, 1\n"
    "s_cbranch_scc0 .Lsp20_%=\n"
    ".Lspd21_%=:\n"
    "s_waitcnt vmcnt(6)\n"
    "buffer_load_dwordx4 v216, s[16:19], s71 offen offset:2048 lds\n"
    "v_and_b32 v196, 0xffff0000, v196\n"
    "v_sub_f32_e64 v226, 1.0, v196\n"
    "v_cvt_pk_bf16_f32 v226, v226, v226\n"
    "v_and_b32 v226, 0xffff0000, v226\n"
    "s_waitcnt vmcnt(6)\n"
    "buffer_load_dwordx4 v217, s[16:19], s71 offen offset:3072 lds\n"
    "s_waitcnt vmcnt(7)\n"
    "ds_read_b64_tr_b16 v[96:97], v204 offset:4096\n"
    "ds_read_b64_tr_b16 v[98:99], v204 offset:4608\n"
    "ds_read_b64_tr_b16 v[100:101], v205 offset:4096\n"
    "ds_read_b64_tr_b16 v[102:103], v205 offset:4608\n"
    "ds_read_b64_tr_b16 v[104:105], v206 offset:4096\n"
    "ds_read_b64_tr_b16 v[106:107], v206 offset:4608\n"
    "ds_read_b64_tr_b16 v[108:109], v207 offset:4096\n"
    "ds_read_b64_tr_b16 v[110:111], v207 offset:4608\n"
    "v_mov_b32 v227, v226\n"
    "v_lshrrev_b32 v196, 16, v196\n"
    "v_lshlrev_b32_e64 v196, v235, v196\n"
    "v_and_b32 v198, 0xffff0000, v198\n"
    "s_waitcnt lgkmcnt(0)\n"
    "s_waitcnt vmcnt(6)\n"
    "s_add_u32 m0, s72, 81920\n"
    "s_add_u32 s71, s76, 8\n"
    "s_lshl_b32 s71, s71, 15\n"
    "buffer_load_dwordx4 v214, s[16:19], s71 offen offset:0 lds\n"
    "v_sub_f32_e64 v228, 1.0, v198\n"
    "v_cvt_pk_bf16_f32 v228, v228, v228\n"
    "v_and_b32 v228, 0xffff0000, v228\n"
    "v_mov_b32 v229, v228\n"
    "s_waitcnt vmcnt(6)\n"
    "buffer_load_dwordx4 v215, s[16:19], s71 offen offset:1024 lds\n"
    "v_lshrrev_b32 v198, 16, v198\n"
    "v_lshlrev_b32 v198, v235, v198\n"
    "v_cmp_eq_u32_e64 s[42:43], 0, v237\n"
    "v_cmp_eq_u32_e64 s[44:45], 1, v237\n"
    "s_waitcnt vmcnt(6)\n"
    "buffer_load_dwordx4 v216, s[16:19], s71 offen offset:2048 lds\n"
    "v_cndmask_b32_e64 v218, 0, v196, s[42:43]\n"
    "v_cndmask_b32_e64 v219, 0, v196, s[44:45]\n"
    "v_cmp_eq_u32_e64 s[42:43], 2, v237\n"
    "v_cmp_eq_u32_e64 s[44:45], 3, v237\n"
    "s_waitcnt vmcnt(6)\n"
    "buffer_load_dwordx4 v217, s[16:19], s71 offen offset:3072 lds\n"
    "s_waitcnt vmcnt(7)\n"
    "ds_read_b64_tr_b16 v[112:113], v200 offset:0\n"
    "ds_read_b64_tr_b16 v[114:115], v200 offset:512\n"
    "ds_read_b64_tr_b16 v[116:117], v201 offset:0\n"
    "ds_read_b64_tr_b16 v[118:119], v201 offset:512\n"
    "ds_read_b64_tr_b16 v[120:121], v202 offset:0\n"
    "ds_read_b64_tr_b16 v[122:123], v202 offset:512\n"
    "ds_read_b64_tr_b16 v[124:125], v203 offset:0\n"
    "ds_read_b64_tr_b16 v[126:127], v203 offset:512\n"
    "v_cndmask_b32_e64 v220, 0, v196, s[42:43]\n"
    "v_cndmask_b32_e64 v221, 0, v196, s[44:45]\n"
    "v_cmp_eq_u32_e64 s[42:43], -8, v237\n"
    "v_cmp_eq_u32_e64 s[44:45], -7, v237\n"
    "s_waitcnt lgkmcnt(0)\n"
    "s_waitcnt vmcnt(6)\n"
    "s_add_u32 m0, s72, 86016\n"
    "s_add_u32 s71, s76, 9\n"
    "s_lshl_b32 s71, s71, 15\n"
    "buffer_load_dwordx4 v214, s[16:19], s71 offen offset:0 lds\n"
    "v_cndmask_b32_e64 v222, 0, v198, s[42:43]\n"
    "v_cndmask_b32_e64 v223, 0, v198, s[44:45]\n"
    "v_cmp_eq_u32_e64 s[42:43], -6, v237\n"
    "v_cmp_eq_u32_e64 s[44:45], -5, v237\n"
    "s_waitcnt vmcnt(6)\n"
    "buffer_load_dwordx4 v215, s[16:19], s71 offen offset:1024 lds\n"
    "v_cndmask_b32_e64 v224, 0, v198, s[42:43]\n"
    "v_cndmask_b32_e64 v225, 0, v198, s[44:45]\n"
    "s_waitcnt vmcnt(6)\n"
    "buffer_load_dwordx4 v216, s[16:19], s71 offen offset:2048 lds\n"
    "s_waitcnt vmcnt(6)\n"
    "buffer_load_dwordx4 v217, s[16:19], s71 offen offset:3072 lds\n"
    "s_waitcnt lgkmcnt(0)\n"
    "s_waitcnt vmcnt(6)\n"
    "s_add_u32 m0, s72, 0\n"
    "s_lshl_b32 s71, s77, 15\n"
    "buffer_load_dwordx4 v214, s[16:19], s71 offen offset:0 lds\n"
    "s_waitcnt vmcnt(6)\n"
    "buffer_load_dwordx4 v215, s[16:19], s71 offen offset:1024 lds\n"
    "s_waitcnt vmcnt(6)\n"
    "buffer_load_dwordx4 v216, s[16:19], s71 offen offset:2048 lds\n"
    "s_waitcnt vmcnt(6)\n"
    "buffer_load_dwordx4 v217, s[16:19], s71 offen offset:3072 lds\n"
    "s_sleep 4\n"
    "v_mov_b32 v12, 0\n"
    "v_mov_b32 v13, 0\n"
    "s_mov_b64 exec, 0x3ff\n"
    "buffer_atomic_or_x2 v[12:13], v248, s[20:23], 0 offen sc0 sc1\n"
    "s_mov_b64 exec, -1\n"
    "s_waitcnt vmcnt(0)\n"
    "v_cmp_ne_u64 vcc, s[46:47], v[12:13]\n"
    "s_and_b64 vcc, vcc, 0x3ff\n"
    "s_cbranch_vccz .Lspd23_%=\n"
    "s_mov_b32 s63, 0x1000000\n"
    ".Lsp22_%=:\n"
    "s_sleep 2\n"
    "v_mov_b32 v12, 0\n"
    "v_mov_b32 v13, 0\n"
    "s_mov_b64 exec, 0x3ff\n"
    "buffer_atomic_or_x2 v[12:13], v248, s[20:23], 0 offen sc0 sc1\n"
    "s_mov_b64 exec, -1\n"
    "s_waitcnt vmcnt(0)\n"
    "v_cmp_ne_u64 vcc, s[46:47], v[12:13]\n"
    "s_and_b64 vcc, vcc, 0x3ff\n"
    "s_cbranch_vccz .Lspd23_%=\n"
    "s_sub_u32 s63, s63, 1\n"
    "s_cbranch_scc0 .Lsp22_%=\n"
    ".Lspd23_%=:\n"
    "s_setprio 0\n"
    "buffer_load_dwordx4 v[128:131], v212, s[20:23], s28 offen offset:0 sc1\n"
    "buffer_load_dwordx4 v[132:135], v212, s[20:23], s28 offen offset:256 sc1\n"
    "buffer_load_dwordx4 v[136:139], v213, s[20:23], s28 offen offset:0 sc1\n"
    "buffer_load_dwordx4 v[140:143], v213, s[20:23], s28 offen offset:256 sc1\n"
    "buffer_load_dwordx4 v[144:147], v212, s[20:23], s29 offen offset:0 sc1\n"
    "buffer_load_dwordx4 v[148:151], v212, s[20:23], s29 offen offset:256 sc1\n"
    "buffer_load_dwordx4 v[152:155], v213, s[20:23], s29 offen offset:0 sc1\n"
    "buffer_load_dwordx4 v[156:159], v213, s[20:23], s29 offen offset:256 sc1\n"
    "s_waitcnt vmcnt(7)\n"
    "v_mfma_f32_16x16x32_bf16 v[0:3], v[64:67], v[128:131], 0\n"
    "v_mfma_f32_16x16x32_bf16 v[4:7], v[68:71], v[128:131], 0\n"
    "buffer_load_dwordx4 v[160:163], v212, s[20:23], s30 offen offset:0 sc1\n"
    "v_mfma_f32_16x16x32_bf16 v[8:11], v[72:75], v[128:131], 0\n"
    "v_mfma_f32_16x16x32_bf16 v[12:15], v[76:79], v[128:131], 0\n"
    "buffer_load_dwordx4 v[164:167], v212, s[20:23], s30 offen offset:256 sc1\n"
    "s_waitcnt vmcnt(8)\n"
    "v_mfma_f32_16x16x32_bf16 v[16:19], v[64:67], v[132:135], 0\n"
    "v_mfma_f32_16x16x32_bf16 v[20:23], v[68:71], v[132:135], 0\n"
    "buffer_load_dwordx4 v[168:171], v213, s[20:23], s30 offen offset:0 sc1\n"
    "v_mfma_f32_16x16x32_bf16 v[24:27], v[72:75], v[132:135], 0\n"
    "v_mfma_f32_16x16x32_bf16 v[28:31], v[76:79], v[132:135], 0\n"
    "buffer_load_dwordx4 v[172:175], v213, s[20:23], s30 offen offset:256 sc1\n"
    "s_waitcnt vmcnt(9)\n"
    "v_mfma_f32_16x16x32_bf16 v[32:35], v[64:67], v[136:139], 0\n"
    "v_mfma_f32_16x16x32_bf16 v[36:39], v[68:71], v[136:139], 0\n"
    "buffer_load_dwordx4 v[176:179], v212, s[20:23], s31 offen offset:0 sc1\n"
    "v_mfma_f32_16x16x32_bf16 v[40:43], v[72:75], v[136:139], 0\n"
    "v_mfma_f32_16x16x32_bf16 v[44:47], v[76:79], v[136:139], 0\n"
    "buffer_load_dwordx4 v[180:183], v212, s[20:23], s31 offen offset:256 sc1\n"
    "s_waitcnt vmcnt(10)\n"
    "v_mfma_f32_16x16x32_bf16 v[48:51], v[64:67], v[140:143], 0\n"
    "v_mfma_f32_16x16x32_bf16 v[52:55], v[68:71], v[140:143], 0\n"
    "buffer_load_dwordx4 v[184:187], v213, s[20:23], s31 offen offset:0 sc1\n"
    "v_mfma_f32_16x16x32_bf16 v[56:59], v[72:75], v[140:143], 0\n"
    "v_mfma_f32_16x16x32_bf16 v[60:63], v[76:79], v[140:143], 0\n"
    "buffer_load_dwordx4 v[188:191], v213, s[20:23], s31 offen offset:256 sc1\n"
    "s_waitcnt vmcnt(11)\n"
    "v_mfma_f32_16x16x32_bf16 v[0:3], v[80:83], v[144:147], v[0:3]\n"
    "ds_read_b64_tr_b16 v[64:65], v200 offset:4096\n"
    "v_mfma_f32_16x16x32_bf16 v[4:7], v[84:87], v[144:147], v[4:7]\n"
    "buffer_load_dwordx4 v[128:131], v212, s[20:23], s32 offen offset:0 sc1\n"
    "v_mfma_f32_16x16x32_bf16 v[8:11], v[88:91], v[144:147], v[8:11]\n"
    "ds_read_b64_tr_b16 v[66:67], v200 offset:4608\n"
    "v_mfma_f32_16x16x32_bf16 v[12:15], v[92:95], v[144:147], v[12:15]\n"
    "buffer_load_dwordx4 v[132:135], v212, s[20:23], s32 offen offset:256 sc1\n"
    "s_waitcnt vmcnt(12)\n"
    "v_mfma_f32_16x16x32_bf16 v[16:19], v[80:83], v[148:151], v[16:19]\n"
    "ds_read_b64_tr_b16 v[68:69], v201 offset:4096\n"
    "v_mfma_f32_16x16x32_bf16 v[20:23], v[84:87], v[148:151], v[20:23]\n"
    "buffer_load_dwordx4 v[136:139], v213, s[20:23], s32 offen offset:0 sc1\n"
    "v_mfma_f32_16x16x32_bf16 v[24:27], v[88:91], v[148:151], v[24:27]\n"
    "ds_read_b64_tr_b16 v[70:71], v201 offset:4608\n"
    "v_mfma_f32_16x16x32_bf16 v[28:31], v[92:95], v[148:151], v[28:31]\n"
    "buffer_load_dwordx4 v[140:143], v213, s[20:23], s32 offen offset:256 sc1\n"
    "s_waitcnt vmcnt(13)\n"
    "v_mfma_f32_16x16x32_bf16 v[32:35], v[80:83], v[152:155], v[32:35]\n"
    "ds_read_b64_tr_b16 v[72:73], v202 offset:4096\n"
    "v_mfma_f32_16x16x32_bf16 v[36:39], v[84:87], v[152:155], v[36:39]\n"
    "buffer_load_dwordx4 v[144:147], v212, s[20:23], s33 offen offset:0 sc1\n"
    "v_mfma_f32_16x16x32_bf16 v[40:43], v[88:91], v[152:155], v[40:43]\n"
    "ds_read_b64_tr_b16 v[74:75], v202 offset:4608\n"
    "v_mfma_f32_16x16x32_bf16 v[44:47], v[92:95], v[152:155], v[44:47]\n"
    "buffer_load_dwordx4 v[148:151], v212, s[20:23], s33 offen offset:256 sc1\n"
    "s_waitcnt vmcnt(14)\n"
    "v_mfma_f32_16x16x32_bf16 v[48:51], v[80:83], v[156:159], v[48:51]\n"
    "ds_read_b64_tr_b16 v[76:77], v203 offset:4096\n"
    "v_mfma_f32_16x16x32_bf16 v[52:55], v[84:87], v[156:159], v[52:55]\n"
    "buffer_load_dwordx4 v[152:155], v213, s[20:23], s33 offen offset:0 sc1\n"
    "v_mfma_f32_16x16x32_bf16 v[56:59], v[88:91], v[156:159], v[56:59]\n"
    "ds_read_b64_tr_b16 v[78:79], v203 offset:4608\n"
    "v_mfma_f32_16x16x32_bf16 v[60:63], v[92:95], v[156:159], v[60:63]\n"
    "buffer_load_dwordx4 v[156:159], v213, s[20:23], s33 offen offset:256 sc1\n"
    "s_waitcnt vmcnt(15)\n"
    "v_mfma_f32_16x16x32_bf16 v[0:3], v[96:99], v[160:163], v[0:3]\n"
    "ds_read_b64_tr_b16 v[80:81], v200 offset:40960\n"
    "v_mfma_f32_16x16x32_bf16 v[4:7], v[100:103], v[160:163], v[4:7]\n"
    "v_mfma_f32_16x16x32_bf16 v[8:11], v[104:107], v[160:163], v[8:11]\n"
    "ds_read_b64_tr_b16 v[82:83], v200 offset:41472\n"
    "v_mfma_f32_16x16x32_bf16 v[12:15], v[108:111], v[160:163], v[12:15]\n"
    "buffer_load_dwordx4 v[160:163], v212, s[20:23], s34 offen offset:0 sc1\n"
    "s_waitcnt vmcnt(15)\n"
    "v_mfma_f32_16x16x32_bf16 v[16:19], v[96:99], v[164:167], v[16:19]\n"
    "ds_read_b64_tr_b16 v[84:85], v201 offset:40960\n"
    "v_mfma_f32_16x16x32_bf16 v[20:23], v[100:103], v[164:167], v[20:23]\n"
    "v_mfma_f32_16x16x32_bf16 v[24:27], v[104:107], v[164:167], v[24:27]\n"
    "ds_read_b64_tr_b16 v[86:87], v201 offset:41472\n"
    "v_mfma_f32_16x16x32_bf16 v[28:31], v[108:111], v[164:167], v[28:31]\n"
    "buffer_load_dwordx4 v[164:167], v212, s[20:23], s34 offen offset:256 sc1\n"
    "s_waitcnt vmcnt(15)\n"
    "v_mfma_f32_16x16x32_bf16 v[32:35], v[96:99], v[168:171], v[32:35]\n"
    "ds_read_b64_tr_b16 v[88:89], v202 offset:40960\n"
    "v_mfma_f32_16x16x32_bf16 v[36:39], v[100:103], v[168:171], v[36:39]\n"
    "v_mfma_f32_16x16x32_bf16 v[40:43], v[104:107], v[168:171], v[40:43]\n"
    "ds_read_b64_tr_b16 v[90:91], v202 offset:41472\n"
    "v_mfma_f32_16x16x32_bf16 v[44:47], v[108:111], v[168:171], v[44:47]\n"
    "buffer_load_dwordx4 v[168:171], v213, s[20:23], s34 offen offset:0 sc1\n"
    "s_waitcnt vmcnt(15)\n"
    "v_mfma_f32_16x16x32_bf16 v[48:51], v[96:99], v[172:175], v[48:51]\n"
    "ds_read_b64_tr_b16 v[92:93], v203 offset:40960\n"
    "v_mfma_f32_16x16x32_bf16 v[52:55], v[100:103], v[172:175], v[52:55]\n"
    "v_mfma_f32_16x16x32_bf16 v[56:59], v[104:107], v[172:175], v[56:59]\n"
    "ds_read_b64_tr_b16 v[94:95], v203 offset:41472\n"
    "v_mfma_f32_16x16x32_bf16 v[60:63], v[108:111], v[172:175], v[60:63]\n"
    "buffer_load_dwordx4 v[172:175], v213, s[20:23], s34 offen offset:256 sc1\n"
    "s_waitcnt vmcnt(15)\n"
    "v_mfma_f32_16x16x32_bf16 v[0:3], v[112:115], v[176:179], v[0:3]\n"
    "ds_read_b64_tr_b16 v[96:97], v200 offset:45056\n"
    "v_mfma_f32_16x16x32_bf16 v[4:7], v[116:119], v[176:179], v[4:7]\n"
    "v_mfma_f32_16x16x32_bf16 v[8:11], v[120:123], v[176:179], v[8:11]\n"
    "ds_read_b64_tr_b16 v[98:99], v200 offset:45568\n"
    "v_mfma_f32_16x16x32_bf16 v[12:15], v[124:127], v[176:179], v[12:15]\n"
    "buffer_load_dwordx4 v[176:179], v212, s[20:23], s35 offen offset:0 sc1\n"
    "s_waitcnt vmcnt(15)\n"
    "v_mfma_f32_16x16x32_bf16 v[16:19], v[112:115], v[180:183], v[16:19]\n"
    "ds_read_b64_tr_b16 v[100:101], v201 offset:45056\n"
    "v_mfma_f32_16x16x32_bf16 v[20:23], v[116:119], v[180:183], v[20:23]\n"
    "v_mfma_f32_16x16x32_bf16 v[24:27], v[120:123], v[180:183], v[24:27]\n"
    "ds_read_b64_tr_b16 v[102:103], v201 offset:45568\n"
    "v_mfma_f32_16x16x32_bf16 v[28:31], v[124:127], v[180:183], v[28:31]\n"
    "buffer_load_dwordx4 v[180:183], v212, s[20:23], s35 offen offset:256 sc1\n"
    "s_waitcnt vmcnt(15)\n"
    "v_mfma_f32_16x16x32_bf16 v[32:35], v[112:115], v[184:187], v[32:35]\n"
    "ds_read_b64_tr_b16 v[104:105], v202 offset:45056\n"
    "v_mfma_f32_16x16x32_bf16 v[36:39], v[116:119], v[184:187], v[36:39]\n"
    "v_mfma_f32_16x16x32_bf16 v[40:43], v[120:123], v[184:187], v[40:43]\n"
    "ds_read_b64_tr_b16 v[106:107], v202 offset:45568\n"
    "v_mfma_f32_16x16x32_bf16 v[44:47], v[124:127], v[184:187], v[44:47]\n"
    "buffer_load_dwordx4 v[184:187], v213, s[20:23], s35 offen offset:0 sc1\n"
    "s_waitcnt vmcnt(15)\n"
    "v_mfma_f32_16x16x32_bf16 v[48:51], v[112:115], v[188:191], v[48:51]\n"
    "ds_read_b64_tr_b16 v[108:109], v203 offset:45056\n"
    "v_mfma_f32_16x16x32_bf16 v[52:55], v[116:119], v[188:191], v[52:55]\n"
    "v_mfma_f32_16x16x32_bf16 v[56:59], v[120:123], v[188:191], v[56:59]\n"
    "ds_read_b64_tr_b16 v[110:111], v203 offset:45568\n"
    "v_mfma_f32_16x16x32_bf16 v[60:63], v[124:127], v[188:191], v[60:63]\n"
    "buffer_load_dwordx4 v[188:191], v213, s[20:23], s35 offen offset:256 sc1\n"
    "s_waitcnt vmcnt(15) lgkmcnt(15)\n"
    "v_mfma_f32_16x16x32_bf16 v[0:3], v[64:67], v[128:131], v[0:3]\n"
    "ds_read_b64_tr_b16 v[112:113], v208 offset:0\n"
    "v_mfma_f32_16x16x32_bf16 v[4:7], v[68:71], v[128:131], v[4:7]\n"
    "v_mfma_f32_16x16x32_bf16 v[8:11], v[72:75], v[128:131], v[8:11]\n"
    "ds_read_b64_tr_b16 v[114:115], v208 offset:512\n"
    "v_mfma_f32_16x16x32_bf16 v[12:15], v[76:79], v[128:131], v[12:15]\n"
    "buffer_load_dwordx4 v[128:131], v212, s[20:23], s36 offen offset:0 sc1\n"
    "s_waitcnt vmcnt(15)\n"
    "v_mfma_f32_16x16x32_bf16 v[16:19], v[64:67], v[132:135], v[16:19]\n"
    "ds_read_b64_tr_b16 v[116:117], v209 offset:0\n"
    "v_mfma_f32_16x16x32_bf16 v[20:23], v[68:71], v[132:135], v[20:23]\n"
    "v_mfma_f32_16x16x32_bf16 v[24:27], v[72:75], v[132:135], v[24:27]\n"
    "ds_read_b64_tr_b16 v[118:119], v209 offset:512\n"
    "v_mfma_f32_16x16x32_bf16 v[28:31], v[76:79], v[132:135], v[28:31]\n"
    "buffer_load_dwordx4 v[132:135], v212, s[20:23], s36 offen offset:256 sc1\n"
    "s_waitcnt vmcnt(15)\n"
    "v_mfma_f32_16x16x32_bf16 v[32:35], v[64:67], v[136:139], v[32:35]\n"
    "ds_read_b64_tr_b16 v[120:121], v210 offset:0\n"
    "v_mfma_f32_16x16x32_bf16 v[36:39], v[68:71], v[136:139], v[36:39]\n"
    "v_mfma_f32_16x16x32_bf16 v[40:43], v[72:75], v[136:139], v[40:43]\n"
    "ds_read_b64_tr_b16 v[122:123], v210 offset:512\n"
    "v_mfma_f32_16x16x32_bf16 v[44:47], v[76:79], v[136:139], v[44:47]\n"
    "buffer_load_dwordx4 v[136:139], v212, s[20:23], s37 offen offset:0 sc1\n"
    "s_waitcnt vmcnt(15)\n"
    "v_mfma_f32_16x16x32_bf16 v[48:51], v[64:67], v[140:143], v[48:51]\n"
    "ds_read_b64_tr_b16 v[124:125], v211 offset:0\n"
    "v_mfma_f32_16x16x32_bf16 v[52:55], v[68:71], v[140:143], v[52:55]\n"
    "v_mfma_f32_16x16x32_bf16 v[56:59], v[72:75], v[140:143], v[56:59]\n"
    "ds_read_b64_tr_b16 v[126:127], v211 offset:512\n"
    "v_mfma_f32_16x16x32_bf16 v[60:63], v[76:79], v[140:143], v[60:63]\n"
    "buffer_load_dwordx4 v[140:143], v212, s[20:23], s37 offen offset:256 sc1\n"
    "s_waitcnt vmcnt(15) lgkmcnt(15)\n"
    "v_mfma_f32_16x16x32_bf16 v[0:3], v[80:83], v[144:147], v[0:3]\n"
    "ds_read_b64_tr_b16 v[64:65], v204 offset:0\n"
    "v_mfma_f32_16x16x32_bf16 v[4:7], v[84:87], v[144:147], v[4:7]\n"
    "v_mfma_f32_16x16x32_bf16 v[8:11], v[88:91], v[144:147], v[8:11]\n"
    "ds_read_b64_tr_b16 v[66:67], v204 offset:512\n"
    "v_mfma_f32_16x16x32_bf16 v[12:15], v[92:95], v[144:147], v[12:15]\n"
    "buffer_load_dwordx4 v[144:147], v212, s[20:23], s38 offen offset:0 sc1\n"
    "s_waitcnt vmcnt(15)\n"
    "v_mfma_f32_16x16x32_bf16 v[16:19], v[80:83], v[148:151], v[16:19]\n"
    "ds_read_b64_tr_b16 v[68:69], v205 offset:0\n"
    "v_mfma_f32_16x16x32_bf16 v[20:23], v[84:87], v[148:151], v[20:23]\n"
    "v_mfma_f32_16x16x32_bf16 v[24:27], v[88:91], v[148:151], v[24:27]\n"
    "ds_read_b64_tr_b16 v[70:71], v205 offset:512\n"
    "v_mfma_f32_16x16x32_bf16 v[28:31], v[92:95], v[148:151], v[28:31]\n"
    "buffer_load_dwordx4 v[148:151], v212, s[20:23], s38 offen offset:256 sc1\n"
    "s_waitcnt vmcnt(15)\n"
    "v_mfma_f32_16x16x32_bf16 v[32:35], v[80:83], v[152:155], v[32:35]\n"
    "ds_read_b64_tr_b16 v[72:73], v206 offset:0\n"
    "v_mfma_f32_16x16x32_bf16 v[36:39], v[84:87], v[152:155], v[36:39]\n"
    "v_mfma_f32_16x16x32_bf16 v[40:43], v[88:91], v[152:155], v[40:43]\n"
    "ds_read_b64_tr_b16 v[74:75], v206 offset:512\n"
    "v_mfma_f32_16x16x32_bf16 v[44:47], v[92:95], v[152:155], v[44:47]\n"
    "buffer_load_dwordx4 v[152:155], v213, s[20:23], s36 offen offset:0 sc1\n"
    "s_waitcnt vmcnt(15)\n"
    "v_mfma_f32_16x16x32_bf16 v[48:51], v[80:83], v[156:159], v[48:51]\n"
    "ds_read_b64_tr_b16 v[76:77], v207 offset:0\n"
    "v_mfma_f32_16x16x32_bf16 v[52:55], v[84:87], v[156:159], v[52:55]\n"
    "v_mfma_f32_16x16x32_bf16 v[56:59], v[88:91], v[156:159], v[56:59]\n"
    "ds_read_b64_tr_b16 v[78:79], v207 offset:512\n"
    "v_mfma_f32_16x16x32_bf16 v[60:63], v[92:95], v[156:159], v[60:63]\n"
    "buffer_load_dwordx4 v[156:159], v213, s[20:23], s36 offen offset:256 sc1\n"
    "s_waitcnt vmcnt(15) lgkmcnt(15)\n"
    "v_mfma_f32_16x16x32_bf16 v[0:3], v[96:99], v[160:163], v[0:3]\n"
    "ds_read_b64_tr_b16 v[80:81], v204 offset:4096\n"
    "v_mfma_f32_16x16x32_bf16 v[4:7], v[100:103], v[160:163], v[4:7]\n"
    "v_mfma_f32_16x16x32_bf16 v[8:11], v[104:107], v[160:163], v[8:11]\n"
    "ds_read_b64_tr_b16 v[82:83], v204 offset:4608\n"
    "v_mfma_f32_16x16x32_bf16 v[12:15], v[108:111], v[160:163], v[12:15]\n"
    "buffer_load_dwordx4 v[160:163], v213, s[20:23], s37 offen offset:0 sc1\n"
    "s_waitcnt vmcnt(15)\n"
    "v_mfma_f32_16x16x32_bf16 v[16:19], v[96:99], v[164:167], v[16:19]\n"
    "ds_read_b64_tr_b16 v[84:85], v205 offset:4096\n"
    "v_mfma_f32_16x16x32_bf16 v[20:23], v[100:103], v[164:167], v[20:23]\n"
    "v_mfma_f32_16x16x32_bf16 v[24:27], v[104:107], v[164:167], v[24:27]\n"
    "ds_read_b64_tr_b16 v[86:87], v205 offset:4608\n"
    "v_mfma_f32_16x16x32_bf16 v[28:31], v[108:111], v[164:167], v[28:31]\n"
    "buffer_load_dwordx4 v[164:167], v213, s[20:23], s37 offen offset:256 sc1\n"
    "buffer_load_dwordx4 v[240:243], v253, s[20:23], s79 offen sc1\n"
    "buffer_load_dwordx4 v[244:247], v253, s[20:23], s80 offen sc1\n"
    "s_waitcnt vmcnt(17)\n"
    "v_mfma_f32_16x16x32_bf16 v[32:35], v[96:99], v[168:171], v[32:35]\n"
    "ds_read_b64_tr_b16 v[88:89], v206 offset:4096\n"
    "v_mfma_f32_16x16x32_bf16 v[36:39], v[100:103], v[168:171], v[36:39]\n"
    "v_mfma_f32_16x16x32_bf16 v[40:43], v[104:107], v[168:171], v[40:43]\n"
    "ds_read_b64_tr_b16 v[90:91], v206 offset:4608\n"
    "v_mfma_f32_16x16x32_bf16 v[44:47], v[108:111], v[168:171], v[44:47]\n"
    "buffer_load_dwordx4 v[168:171], v213, s[20:23], s38 offen offset:0 sc1\n"
    "s_waitcnt vmcnt(17)\n"
    "v_mfma_f32_16x16x32_bf16 v[48:51], v[96:99], v[172:175], v[48:51]\n"
    "ds_read_b64_tr_b16 v[92:93], v207 offset:4096\n"
    "v_mfma_f32_16x16x32_bf16 v[52:55], v[100:103], v[172:175], v[52:55]\n"
    "v_mfma_f32_16x16x32_bf16 v[56:59], v[104:107], v[172:175], v[56:59]\n"
    "ds_read_b64_tr_b16 v[94:95], v207 offset:4608\n"
    "v_mfma_f32_16x16x32_bf16 v[60:63], v[108:111], v[172:175], v[60:63]\n"
    "buffer_load_dwordx4 v[172:175], v213, s[20:23], s38 offen offset:256 sc1\n"
    "s_waitcnt vmcnt(17) lgkmcnt(15)\n"
    "v_mfma_f32_16x16x32_bf16 v[0:3], v[112:115], v[176:179], v[0:3]\n"
    "ds_read_b64_tr_b16 v[96:97], v200 offset:0\n"
    "v_mfma_f32_16x16x32_bf16 v[4:7], v[116:119], v[176:179], v[4:7]\n"
    "v_mfma_f32_16x16x32_bf16 v[8:11], v[120:123], v[176:179], v[8:11]\n"
    "ds_read_b64_tr_b16 v[98:99], v200 offset:512\n"
    "v_mfma_f32_16x16x32_bf16 v[12:15], v[124:127], v[176:179], v[12:15]\n"
    "s_waitcnt vmcnt(16)\n"
    "v_mfma_f32_16x16x32_bf16 v[16:19], v[112:115], v[180:183], v[16:19]\n"
    "ds_read_b64_tr_b16 v[100:101], v201 offset:0\n"
    "v_mfma_f32_16x16x32_bf16 v[20:23], v[116:119], v[180:183], v[20:23]\n"
    "v_mfma_f32_16x16x32_bf16 v[24:27], v[120:123], v[180:183], v[24:27]\n"
    "ds_read_b64_tr_b16 v[102:103], v201 offset:512\n"
    "v_mfma_f32_16x16x32_bf16 v[28:31], v[124:127], v[180:183], v[28:31]\n"
    "s_waitcnt vmcnt(15)\n"
    "v_mfma_f32_16x16x32_bf16 v[32:35], v[112:115], v[184:187], v[32:35]\n"
    "ds_read_b64_tr_b16 v[104:105], v202 offset:0\n"
    "v_mfma_f32_16x16x32_bf16 v[36:39], v[116:119], v[184:187], v[36:39]\n"
    "v_mfma_f32_16x16x32_bf16 v[40:43], v[120:123], v[184:187], v[40:43]\n"
    "ds_read_b64_tr_b16 v[106:107], v202 offset:512\n"
    "v_mfma_f32_16x16x32_bf16 v[44:47], v[124:127], v[184:187], v[44:47]\n"
    "s_waitcnt vmcnt(14)\n"
    "v_mfma_f32_16x16x32_bf16 v[48:51], v[112:115], v[188:191], v[48:51]\n"
    "ds_read_b64_tr_b16 v[108:109], v203 offset:0\n"
    "v_mfma_f32_16x16x32_bf16 v[52:55], v[116:119], v[188:191], v[52:55]\n"
    "v_mfma_f32_16x16x32_bf16 v[56:59], v[120:123], v[188:191], v[56:59]\n"
    "ds_read_b64_tr_b16 v[110:111], v203 offset:512\n"
    "v_mfma_f32_16x16x32_bf16 v[60:63], v[124:127], v[188:191], v[60:63]\n"
    "s_waitcnt vmcnt(13) lgkmcnt(15)\n"
    "v_mfma_f32_16x16x32_bf16 v[0:3], v[64:67], v[128:131], v[0:3]\n"
    "v_mfma_f32_16x16x32_bf16 v[4:7], v[68:71], v[128:131], v[4:7]\n"
    "v_mfma_f32_16x16x32_bf16 v[8:11], v[72:75], v[128:131], v[8:11]\n"
    "v_mfma_f32_16x16x32_bf16 v[12:15], v[76:79], v[128:131], v[12:15]\n"
    "s_waitcnt vmcnt(12)\n"
    "v_mfma_f32_16x16x32_bf16 v[16:19], v[64:67], v[132:135], v[16:19]\n"
    "v_mfma_f32_16x16x32_bf16 v[20:23], v[68:71], v[132:135], v[20:23]\n"
    "v_mfma_f32_16x16x32_bf16 v[24:27], v[72:75], v[132:135], v[24:27]\n"
    "v_mfma_f32_16x16x32_bf16 v[28:31], v[76:79], v[132:135], v[28:31]\n"
    "s_waitcnt vmcnt(11) lgkmcnt(14)\n"
    "v_mfma_f32_16x16x32_bf16 v[0:3], v[80:83], v[136:139], v[0:3]\n"
    "s_waitcnt lgkmcnt(12)\n"
    "v_mfma_f32_16x16x32_bf16 v[4:7], v[84:87], v[136:139], v[4:7]\n"
    "s_waitcnt lgkmcnt(10)\n"
    "v_mfma_f32_16x16x32_bf16 v[8:11], v[88:91], v[136:139], v[8:11]\n"
    "s_waitcnt lgkmcnt(8)\n"
    "v_mfma_f32_16x16x32_bf16 v[12:15], v[92:95], v[136:139], v[12:15]\n"
    "s_waitcnt vmcnt(10)\n"
    "v_mfma_f32_16x16x32_bf16 v[16:19], v[80:83], v[140:143], v[16:19]\n"
    "v_mfma_f32_16x16x32_bf16 v[20:23], v[84:87], v[140:143], v[20:23]\n"
    "v_mfma_f32_16x16x32_bf16 v[24:27], v[88:91], v[140:143], v[24:27]\n"
    "v_mfma_f32_16x16x32_bf16 v[28:31], v[92:95], v[140:143], v[28:31]\n"
    "s_waitcnt vmcnt(9) lgkmcnt(6)\n"
    "v_mfma_f32_16x16x32_bf16 v[0:3], v[96:99], v[144:147], v[0:3]\n"
    "s_waitcnt lgkmcnt(4)\n"
    "v_mfma_f32_16x16x32_bf16 v[4:7], v[100:103], v[144:147], v[4:7]\n"
    "s_waitcnt lgkmcnt(2)\n"
    "v_mfma_f32_16x16x32_bf16 v[8:11], v[104:107], v[144:147], v[8:11]\n"
    "s_waitcnt lgkmcnt(0)\n"
    "v_mfma_f32_16x16x32_bf16 v[12:15], v[108:111], v[144:147], v[12:15]\n"
    "s_waitcnt vmcnt(8)\n"
    "v_mfma_f32_16x16x32_bf16 v[16:19], v[96:99], v[148:151], v[16:19]\n"
    "v_mfma_f32_16x16x32_bf16 v[20:23], v[100:103], v[148:151], v[20:23]\n"
    "v_mfma_f32_16x16x32_bf16 v[24:27], v[104:107], v[148:151], v[24:27]\n"
    "v_mfma_f32_16x16x32_bf16 v[28:31], v[108:111], v[148:151], v[28:31]\n"
    "s_waitcnt vmcnt(7)\n"
    "v_mfma_f32_16x16x32_bf16 v[32:35], v[64:67], v[152:155], v[32:35]\n"
    "v_mfma_f32_16x16x32_bf16 v[36:39], v[68:71], v[152:155], v[36:39]\n"
    "ds_write_b128 v230, v[0:3] offset:40960\n"
    "v_mfma_f32_16x16x32_bf16 v[40:43], v[72:75], v[152:155], v[40:43]\n"
    "ds_write_b128 v230, v[16:19] offset:41984\n"
    "v_mfma_f32_16x16x32_bf16 v[44:47], v[76:79], v[152:155], v[44:47]\n"
    "ds_write_b128 v230, v[4:7] offset:43008\n"
    "s_waitcnt vmcnt(6)\n"
    "v_mfma_f32_16x16x32_bf16 v[48:51], v[64:67], v[156:159], v[48:51]\n"
    "v_mfma_f32_16x16x32_bf16 v[52:55], v[68:71], v[156:159], v[52:55]\n"
    "v_mfma_f32_16x16x32_bf16 v[56:59], v[72:75], v[156:159], v[56:59]\n"
    "v_mfma_f32_16x16x32_bf16 v[60:63], v[76:79], v[156:159], v[60:63]\n"
    "s_waitcnt vmcnt(5)\n"
    "v_mfma_f32_16x16x32_bf16 v[32:35], v[80:83], v[160:163], v[32:35]\n"
    "v_mfma_f32_16x16x32_bf16 v[36:39], v[84:87], v[160:163], v[36:39]\n"
    "ds_write_b128 v230, v[20:23] offset:44032\n"
    "v_mfma_f32_16x16x32_bf16 v[40:43], v[88:91], v[160:163], v[40:43]\n"
    "ds_write_b128 v231, v[8:11] offset:40960\n"
    "v_mfma_f32_16x16x32_bf16 v[44:47], v[92:95], v[160:163], v[44:47]\n"
    "ds_write_b128 v231, v[24:27] offset:41984\n"
    "s_waitcnt vmcnt(4)\n"
    "v_mfma_f32_16x16x32_bf16 v[48:51], v[80:83], v[164:167], v[48:51]\n"
    "v_mfma_f32_16x16x32_bf16 v[52:55], v[84:87], v[164:167], v[52:55]\n"
    "v_mfma_f32_16x16x32_bf16 v[56:59], v[88:91], v[164:167], v[56:59]\n"
    "v_mfma_f32_16x16x32_bf16 v[60:63], v[92:95], v[164:167], v[60:63]\n"
    "s_waitcnt vmcnt(3)\n"
    "v_max3_u32 v238, v240, v241, v242\n"
    "s_waitcnt vmcnt(2)\n"
    "v_max3_u32 v239, v243, v244, v245\n"
    "v_max3_u32 v238, v238, v246, v247\n"
    "v_max_u32 v238, v238, v239\n"
    "v_cmp_eq_u32 vcc, -1, v238\n"
    "s_cbranch_vccz .Lspd25_%=\n"
    "s_mov_b32 s63, 0x1000000\n"
    ".Lsp24_%=:\n"
    "s_sleep 6\n"
    "buffer_load_dwordx4 v[240:243], v253, s[20:23], s79 offen sc1\n"
    "buffer_load_dwordx4 v[244:247], v253, s[20:23], s80 offen sc1\n"
    "s_waitcnt vmcnt(0)\n"
    "v_max3_u32 v238, v240, v241, v242\n"
    "v_max3_u32 v239, v243, v244, v245\n"
    "v_max3_u32 v238, v238, v246, v247\n"
    "v_max_u32 v238, v238, v239\n"
    "v_cmp_eq_u32 vcc, -1, v238\n"
    "s_cbranch_vccz .Lspd25_%=\n"
    "s_sub_u32 s63, s63, 1\n"
    "s_cbranch_scc0 .Lsp24_%=\n"
    ".Lspd25_%=:\n"
    "v_mfma_f32_16x16x32_bf16 v[112:115], v[240:243], v[218:221], 0\n"
    "v_mfma_f32_16x16x32_bf16 v[120:123], v[240:243], v[222:225], 0\n"
    "v_mfma_f32_16x16x32_bf16 v[116:119], v[244:247], v[218:221], 0\n"
    "v_mfma_f32_16x16x32_bf16 v[124:127], v[244:247], v[222:225], 0\n"
    "s_waitcnt vmcnt(1)\n"
    "v_mfma_f32_16x16x32_bf16 v[40:43], v[104:107], v[168:171], v[40:43]\n"
    "v_mfma_f32_16x16x32_bf16 v[44:47], v[108:111], v[168:171], v[44:47]\n"
    "ds_write_b128 v231, v[12:15] offset:43008\n"
    "s_waitcnt vmcnt(0)\n"
    "v_mfma_f32_16x16x32_bf16 v[56:59], v[104:107], v[172:175], v[56:59]\n"
    "ds_write_b128 v231, v[28:31] offset:44032\n"
    "v_mfma_f32_16x16x32_bf16 v[60:63], v[108:111], v[172:175], v[60:63]\n"
    "v_mfma_f32_16x16x32_bf16 v[32:35], v[96:99], v[168:171], v[32:35]\n"
    "v_mfma_f32_16x16x32_bf16 v[36:39], v[100:103], v[168:171], v[36:39]\n"
    "v_mfma_f32_16x16x32_bf16 v[48:51], v[96:99], v[172:175], v[48:51]\n"
    "v_mfma_f32_16x16x32_bf16 v[52:55], v[100:103], v[172:175], v[52:55]\n"
    "ds_write_b128 v232, v[40:43] offset:4096\n"
    "ds_write_b128 v232, v[44:47] offset:6144\n"
    "ds_write_b128 v232, v[56:59] offset:5120\n"
    "s_nop 0\n"
    "s_nop 0\n"
    "ds_write_b128 v232, v[60:63] offset:7168\n"
    "v_pk_fma_f32 v[32:33], v[32:33], v[226:227], v[112:113]\n"
    "v_pk_fma_f32 v[34:35], v[34:35], v[226:227], v[114:115]\n"
    "v_pk_fma_f32 v[36:37], v[36:37], v[226:227], v[116:117]\n"
    "v_pk_fma_f32 v[38:39], v[38:39], v[226:227], v[118:119]\n"
    "v_pk_fma_f32 v[48:49], v[48:49], v[228:229], v[120:121]\n"
    "v_pk_fma_f32 v[50:51], v[50:51], v[228:229], v[122:123]\n"
    "v_pk_fma_f32 v[52:53], v[52:53], v[228:229], v[124:125]\n"
    "v_pk_fma_f32 v[54:55], v[54:55], v[228:229], v[126:127]\n"
    "s_waitcnt lgkmcnt(0)\n"
    "s_barrier\n"
    "ds_read_b128 v[64:67], v233 offset:40960\n"
    "ds_read_b128 v[80:83], v233 offset:57344\n"
    "ds_read_b128 v[96:99], v234 offset:4096\n"
    "ds_read_b128 v[68:71], v233 offset:41984\n"
    "ds_read_b128 v[84:87], v233 offset:58368\n"
    "ds_read_b128 v[100:103], v234 offset:5120\n"
    "ds_read_b128 v[72:75], v233 offset:43008\n"
    "ds_read_b128 v[88:91], v233 offset:59392\n"
    "ds_read_b128 v[104:107], v234 offset:6144\n"
    "ds_read_b128 v[76:79], v233 offset:44032\n"
    "ds_read_b128 v[92:95], v233 offset:60416\n"
    "ds_read_b128 v[108:111], v234 offset:7168\n"
    "s_waitcnt lgkmcnt(10)\n"
    "v_pk_add_f32 v[64:65], v[64:65], v[80:81]\n"
    "v_pk_add_f32 v[66:67], v[66:67], v[82:83]\n"
    "s_waitcnt lgkmcnt(9)\n"
    "v_pk_add_f32 v[64:65], v[64:65], v[96:97]\n"
    "v_pk_add_f32 v[66:67], v[66:67], v[98:99]\n"
    "v_pk_fma_f32 v[32:33], v[64:65], v[226:227], v[32:33]\n"
    "v_pk_fma_f32 v[34:35], v[66:67], v[226:227], v[34:35]\n"
    "v_cvt_pk_bf16_f32 v136, v32, v33\n"
    "v_cvt_pk_bf16_f32 v137, v34, v35\n"
    "s_waitcnt lgkmcnt(7)\n"
    "v_pk_add_f32 v[68:69], v[68:69], v[84:85]\n"
    "v_pk_add_f32 v[70:71], v[70:71], v[86:87]\n"
    "s_waitcnt lgkmcnt(6)\n"
    "v_pk_add_f32 v[68:69], v[68:69], v[100:101]\n"
    "v_pk_add_f32 v[70:71], v[70:71], v[102:103]\n"
    "v_pk_fma_f32 v[48:49], v[68:69], v[228:229], v[48:49]\n"
    "v_pk_fma_f32 v[50:51], v[70:71], v[228:229], v[50:51]\n"
    "v_cvt_pk_bf16_f32 v138, v48, v49\n"
    "v_cvt_pk_bf16_f32 v139, v50, v51\n"
    "s_nop 0\n"
    "v_permlane16_swap_b32 v136, v138\n"
    "v_permlane16_swap_b32 v137, v139\n"
    "buffer_store_dwordx4 v[136:139], v236, s[24:27], s41 offen offset:0\n"
    "s_waitcnt lgkmcnt(4)\n"
    "v_pk_add_f32 v[72:73], v[72:73], v[88:89]\n"
    "v_pk_add_f32 v[74:75], v[74:75], v[90:91]\n"
    "s_waitcnt lgkmcnt(3)\n"
    "s_nop 0\n"
    "v_pk_add_f32 v[72:73], v[72:73], v[104:105]\n"
    "v_pk_add_f32 v[74:75], v[74:75], v[106:107]\n"
    "v_pk_fma_f32 v[36:37], v[72:73], v[226:227], v[36:37]\n"
    "v_pk_fma_f32 v[38:39], v[74:75], v[226:227], v[38:39]\n"
    "v_cvt_pk_bf16_f32 v140, v36, v37\n"
    "v_cvt_pk_bf16_f32 v141, v38, v39\n"
    "s_waitcnt lgkmcnt(1)\n"
    "v_pk_add_f32 v[76:77], v[76:77], v[92:93]\n"
    "v_pk_add_f32 v[78:79], v[78:79], v[94:95]\n"
    "s_waitcnt lgkmcnt(0)\n"
    "v_pk_add_f32 v[76:77], v[76:77], v[108:109]\n"
    "v_pk_add_f32 v[78:79], v[78:79], v[110:111]\n"
    "v_pk_fma_f32 v[52:53], v[76:77], v[228:229], v[52:53]\n"
    "v_pk_fma_f32 v[54:55], v[78:79], v[228:229], v[54:55]\n"
    "v_cvt_pk_bf16_f32 v142, v52, v53\n"
    "v_cvt_pk_bf16_f32 v143, v54, v55\n"
    "s_nop 0\n"
    "v_permlane16_swap_b32 v140, v142\n"
    "v_permlane16_swap_b32 v141, v143\n"
    "buffer_store_dwordx4 v[140:143], v236, s[24:27], s41 offen offset:32\n"
    "s_endpgm\n"
    ".p2align 3\n"
    ".LM1_%=:\n"
    "s_and_b32 s64, s48, 1\n"
    "s_lshr_b32 s65, s48, 1\n"
    "v_and_b32 v24, 15, v255\n"
    "v_lshrrev_b32 v25, 4, v255\n"
    "v_bfe_u32 v27, v255, 2, 2\n"
    "v_and_b32 v28, 3, v255\n"
    "v_and_b32 v29, 1, v25\n"
    "v_lshlrev_b32 v30, 4, v255\n"
    "v_lshrrev_b32 v26, 1, v24\n"
    "v_xor_b32 v26, v26, v25\n"
    "v_lshlrev_b32 v26, 4, v26\n"
    "v_lshl_or_b32 v26, v24, 7, v26\n"
    "s_lshl_b32 s58, s64, 13\n"
    "v_add_u32 v224, s58, v26\n"
    "v_xor_b32_e64 v225, 64, v224\n"
    "v_add_u32 v226, 81920, v224\n"
    "v_add_u32 v227, 81920, v225\n"
    "v_lshl_add_u32 v32, v25, 3, v27\n"
    "v_mul_u32_u24 v26, 192, v32\n"
    "v_lshl_add_u32 v26, v28, 3, v26\n"
    "s_mul_i32 s58, s65, 96\n"
    "s_add_u32 s58, s58, 16384\n"
    "v_add_u32 v26, s58, v26\n"
    "s_lshl_b32 s58, s65, 6\n"
    "s_sub_u32 s58, 32, s58\n"
    "v_mul_i32_i24 v31, s58, v29\n"
    "v_add_u32 v228, v26, v31\n"
    "v_sub_u32 v229, v26, v31\n"
    "v_add_u32 v230, 81920, v228\n"
    "v_add_u32 v231, 81920, v229\n"
    "v_add_u32 v240, 4718592, v30\n"
    "s_mul_i32 s58, s48, 4608\n"
    "s_add_u32 s58, s58, 145408\n"
    "v_add_u32 v16, s58, v30\n"
    "v_lshrrev_b32 v34, 1, v28\n"
    "v_and_b32_e64 v35, 1, v28\n"
    "v_mul_u32_u24 v36, 1152, v34\n"
    "v_lshl_add_u32 v36, v35, 3, v36\n"
    "v_and_b32_e64 v37, 60, v255\n"
    "v_lshl_add_u32 v36, v37, 2, v36\n"
    "v_add_u32 v17, s58, v36\n"
    "v_lshlrev_b32 v36, 5, v24\n"
    "v_lshl_or_b32 v36, v29, 4, v36\n"
    "v_lshl_or_b32 v36, v3, 9, v36\n"
    "v_add_u32 v18, s58, v36\n"
    "v_sub_u32 v36, 4, v3\n"
    "v_lshlrev_b32_e64 v36, v36, v24\n"
    "v_lshl_add_u32 v36, v25, 8, v36\n"
    "s_add_u32 s58, s58, 1024\n"
    "v_add_u32_e64 v19, s58, v36\n"
    "v_mul_u32_u24 v36, 120, v3\n"
    "v_add3_u32 v20, v19, v36, 8\n"
    "v_lshlrev_b32_e64 v36, 6, v24\n"
    "v_lshl_or_b32 v36, v25, 4, v36\n"
    "s_add_u32 s58, s58, 1024\n"
    "v_add_u32 v21, s58, v36\n"
    "v_lshlrev_b32 v37, 3, v35\n"
    "v_lshrrev_b32_e64 v38, 3, v255\n"
    "v_mov_b32 v246, 0xbdd2d3e8\n"
    "v_mov_b32 v247, 0xc0135761\n"
    "s_lshl1_add_u32 s58, s49, s64\n"
    "s_mul_i32 s59, s58, 172\n"
    "s_mul_i32 s60, s65, 3\n"
    "s_lshl1_add_u32 s59, s52, s59\n"
    "s_add_u32 s59, s59, s60\n"
    "s_lshl_b32 s66, s59, 10\n"
    "s_lshl_b32 s59, s53, 1\n"
    "s_sub_u32 s59, s59, s60\n"
    "s_add_u32 s67, s66, 1024\n"
    "s_add_u32 s68, s66, 2048\n"
    "s_cmp_ge_u32 s59, 3\n"
    "s_cselect_b32 s67, s67, 1073741824\n"
    "s_cselect_b32 s68, s68, 1073741824\n"
    "s_add_u32 s69, s66, 88064\n"
    "s_add_u32 s70, s67, 88064\n"
    "s_add_u32 s71, s68, 88064\n"
    "s_lshl4_add_u32 s58, s58, s50\n"
    "s_lshl1_add_u32 s58, s58, s65\n"
    "s_lshl_b32 s58, s58, 7\n"
    "s_add_u32 s72, s58, 131072\n"
    "s_mov_b32 s76, 0\n"
    "s_mov_b32 s77, 0x80000000\n"
    "s_mul_i32 s58, s65, 4352\n"
    "s_add_u32 s58, s58, 122888\n"
    "v_mul_u32_u24 v36, 1088, v28\n"
    "v_lshl_add_u32 v33, v25, 1, v27\n"
    "v_lshlrev_b32_e64 v33, 3, v33\n"
    "v_add3_u32 v248, v36, v33, s58\n"
    "s_lshl4_add_u32 s58, s65, 131328\n"
    "v_sub_u32 v36, v28, v32\n"
    "v_add_u32 v36, 56, v36\n"
    "v_and_b32 v33, 1, v36\n"
    "v_lshrrev_b32 v36, 1, v36\n"
    "v_lshl_or_b32 v36, v36, 2, v33\n"
    "v_lshl_add_u32 v249, v36, 3, s58\n"
    "s_lshl1_add_u32 s58, s2, s65\n"
    "s_mul_i32 s58, s58, 4224\n"
    "s_add_u32 s58, s58, 2490368\n"
    "v_and_b32 v36, 7, v255\n"
    "v_lshlrev_b32_e64 v33, 4, v38\n"
    "v_lshl_or_b32 v36, v36, 7, v33\n"
    "v_add_u32 v250, s58, v36\n"
    "s_lshl_b32 s58, s48, 11\n"
    "s_add_u32 s58, s58, 32768\n"
    "v_add_u32_e64 v251, s58, v30\n"
    "v_lshl_or_b32 v36, v25, 1, v34\n"
    "v_lshl_or_b32 v36, v36, 3, v27\n"
    "v_lshl_or_b32 v36, v36, 4, v37\n"
    "v_add_u32 v252, s58, v36\n"
    "s_lshl4_add_u32 s58, s48, 141312\n"
    "v_mov_b32_e64 v253, s58\n"
    "s_xor_b32 s58, s58, 20\n"
    "v_lshl_add_u32 v36, v255, 2, s58\n"
    "v_cmp_gt_u32 vcc, 3, v255\n"
    "v_mov_b32 v33, 141388\n"
    "v_mov_b32_e64 v243, 0\n"
    "v_cndmask_b32 v254, v33, v36, vcc\n"
    "v_writelane_b32 v243, 1.0, 0\n"
    "v_add_u32 v36, -1, v29\n"
    "v_and_b32 v33, 31, v255\n"
    "v_lshlrev_b32_e64 v33, 2, v33\n"
    "v_add_u32 v33, 141312, v33\n"
    "ds_write_b32 v33, v36\n"
    "v_mul_u32_u24 v36, 1088, v38\n"
    "v_and_b32_e64 v33, 7, v255\n"
    "v_lshl_add_u32 v36, v33, 3, v36\n"
    "v_add_u32 v36, 122880, v36\n"
    "v_lshlrev_b32_e64 v33, 2, v255\n"
    "v_add_u32 v33, 131584, v33\n"
    "v_mov_b32 v22, 0\n"
    "v_mov_b32 v23, 0\n"
    "ds_write_b64 v36, v[22:23]\n"
    "ds_write_b32 v33, v22\n"
    "s_bitcmp1_b32 s48, 1\n"
    "s_cselect_b32 s58, 0, 16\n"
    "s_waitcnt lgkmcnt(0)\n"
    "s_lshl_b32 s74, s74, s58\n"
    "s_and_b32 s74, s74, 0xffff0000\n"
    "s_lshl_b32 s75, s75, s58\n"
    "s_and_b32 s75, s75, 0xffff0000\n"
    "s_bitcmp1_b32 s48, 0\n"
    "s_cselect_b32 s75, 0, s75\n"
    "s_add_u32 s46, s46, 1\n"
    "s_addc_u32 s47, s47, 0\n"
    "v_mov_b32 v14, 0x3fb8aa3b\n"
    "v_mul_f32 v15, s74, v14\n"
    "v_exp_f32 v15, v15\n"
    "v_mov_b32 v88, 0\n"
    "v_mov_b32_e64 v89, 0\n"
    "v_cvt_pk_bf16_f32 v15, v15, v15\n"
    "v_and_b32 v15, 0xffff0000, v15\n"
    "v_mov_b32 v90, 0\n"
    "v_mov_b32 v91, 0\n"
    "v_writelane_b32 v88, s75, 0\n"
    "v_mov_b32 v244, s46\n"
    "v_mov_b32 v245, s47\n"
    "v_mov_b32 v50, 1.0\n"
    "v_mov_b32 v51, 0\n"
    "s_waitcnt vmcnt(5)\n"
    "v_lshlrev_b32 v8, 16, v8\n"
    "s_waitcnt vmcnt(4)\n"
    "v_lshlrev_b32 v9, 16, v9\n"
    "v_mul_f32 v8, v15, v8\n"
    "v_mul_f32 v9, v15, v9\n"
    "v_mul_f32 v8, v14, v8\n"
    "v_mul_f32 v9, 0x3e22f983, v9\n"
    "v_mul_f32 v32, 0x41000000, v8\n"
    "v_mul_f32 v33, 0x43000000, v8\n"
    "v_mul_f32 v34, 0x41000000, v9\n"
    "v_mul_f32 v35, 0x43000000, v9\n"
    "v_fract_f32 v34, v34\n"
    "v_fract_f32 v35, v35\n"
    "v_fract_f32 v9, v9\n"
    "v_exp_f32 v32, v32\n"
    "v_exp_f32 v33, v33\n"
    "v_exp_f32 v8, v8\n"
    "v_cos_f32 v42, v34\n"
    "v_sin_f32 v43, v34\n"
    "v_cos_f32 v44, v35\n"
    "v_sin_f32 v45, v35\n"
    "v_cos_f32 v40, v9\n"
    "v_sin_f32 v41, v9\n"
    "v_pk_mul_f32 v[42:43], v[42:43], v[32:33] op_sel_hi:[1,0]\n"
    "v_pk_mul_f32 v[44:45], v[44:45], v[32:33] op_sel:[0,1]\n"
    "v_pk_mul_f32 v[40:41], v[40:41], v[8:9] op_sel_hi:[1,0]\n"
    "v_cvt_pk_bf16_f32 v72, v50, v42\n"
    "v_cvt_pk_bf16_f32 v76, v51, v43\n"
    "v_pk_mul_f32 v[52:53], v[42:43], v[42:43] op_sel_hi:[1,0]\n"
    "v_pk_fma_f32 v[52:53], v[42:43], v[42:43], v[52:53] op_sel:[1,1,0] op_sel_hi:[0,1,1] neg_lo:[0,1,0]\n"
    "v_pk_mul_f32 v[54:55], v[52:53], v[42:43] op_sel_hi:[1,0]\n"
    "v_pk_fma_f32 v[54:55], v[52:53], v[42:43], v[54:55] op_sel:[1,1,0] op_sel_hi:[0,1,1] neg_lo:[0,1,0]\n"
    "v_cvt_pk_bf16_f32 v73, v52, v54\n"
    "v_cvt_pk_bf16_f32 v77, v53, v55\n"
    "v_pk_mul_f32 v[52:53], v[54:55], v[42:43] op_sel_hi:[1,0]\n"
    "v_pk_fma_f32 v[52:53], v[54:55], v[42:43], v[52:53] op_sel:[1,1,0] op_sel_hi:[0,1,1] neg_lo:[0,1,0]\n"
    "v_pk_mul_f32 v[54:55], v[52:53], v[42:43] op_sel_hi:[1,0]\n"
    "v_pk_fma_f32 v[54:55], v[52:53], v[42:43], v[54:55] op_sel:[1,1,0] op_sel_hi:[0,1,1] neg_lo:[0,1,0]\n"
    "v_cvt_pk_bf16_f32 v74, v52, v54\n"
    "v_cvt_pk_bf16_f32 v78, v53, v55\n"
    "v_pk_mul_f32 v[52:53], v[54:55], v[42:43] op_sel_hi:[1,0]\n"
    "v_pk_fma_f32 v[52:53], v[54:55], v[42:43], v[52:53] op_sel:[1,1,0] op_sel_hi:[0,1,1] neg_lo:[0,1,0]\n"
    "v_pk_mul_f32 v[54:55], v[52:53], v[42:43] op_sel_hi:[1,0]\n"
    "v_pk_fma_f32 v[54:55], v[52:53], v[42:43], v[54:55] op_sel:[1,1,0] op_sel_hi:[0,1,1] neg_lo:[0,1,0]\n"
    "v_cvt_pk_bf16_f32 v75, v52, v54\n"
    "v_cvt_pk_bf16_f32 v79, v53, v55\n"
    "ds_write_b128 v16, v[72:75] offset:0\n"
    "ds_write_b128 v16, v[76:79] offset:2304\n"
    "v_pk_mul_f32 v[52:53], v[54:55], v[42:43] op_sel_hi:[1,0]\n"
    "v_pk_fma_f32 v[52:53], v[54:55], v[42:43], v[52:53] op_sel:[1,1,0] op_sel_hi:[0,1,1] neg_lo:[0,1,0]\n"
    "v_pk_mul_f32 v[54:55], v[52:53], v[42:43] op_sel_hi:[1,0]\n"
    "v_pk_fma_f32 v[54:55], v[52:53], v[42:43], v[54:55] op_sel:[1,1,0] op_sel_hi:[0,1,1] neg_lo:[0,1,0]\n"
    "v_cvt_pk_bf16_f32 v80, v52, v54\n"
    "v_cvt_pk_bf16_f32 v84, v53, v55\n"
    "v_pk_mul_f32 v[52:53], v[54:55], v[42:43] op_sel_hi:[1,0]\n"
    "v_pk_fma_f32 v[52:53], v[54:55], v[42:43], v[52:53] op_sel:[1,1,0] op_sel_hi:[0,1,1] neg_lo:[0,1,0]\n"
    "v_pk_mul_f32 v[54:55], v[52:53], v[42:43] op_sel_hi:[1,0]\n"
    "v_pk_fma_f32 v[54:55], v[52:53], v[42:43], v[54:55] op_sel:[1,1,0] op_sel_hi:[0,1,1] neg_lo:[0,1,0]\n"
    "v_cvt_pk_bf16_f32 v81, v52, v54\n"
    "v_cvt_pk_bf16_f32 v85, v53, v55\n"
    "v_pk_mul_f32 v[52:53], v[54:55], v[42:43] op_sel_hi:[1,0]\n"
    "v_pk_fma_f32 v[52:53], v[54:55], v[42:43], v[52:53] op_sel:[1,1,0] op_sel_hi:[0,1,1] neg_lo:[0,1,0]\n"
    "v_pk_mul_f32 v[54:55], v[52:53], v[42:43] op_sel_hi:[1,0]\n"
    "v_pk_fma_f32 v[54:55], v[52:53], v[42:43], v[54:55] op_sel:[1,1,0] op_sel_hi:[0,1,1] neg_lo:[0,1,0]\n"
    "v_cvt_pk_bf16_f32 v82, v52, v54\n"
    "v_cvt_pk_bf16_f32 v86, v53, v55\n"
    "v_pk_mul_f32 v[52:53], v[54:55], v[42:43] op_sel_hi:[1,0]\n"
    "v_pk_fma_f32 v[52:53], v[54:55], v[42:43], v[52:53] op_sel:[1,1,0] op_sel_hi:[0,1,1] neg_lo:[0,1,0]\n"
    "v_pk_mul_f32 v[54:55], v[52:53], v[42:43] op_sel_hi:[1,0]\n"
    "v_pk_fma_f32 v[54:55], v[52:53], v[42:43], v[54:55] op_sel:[1,1,0] op_sel_hi:[0,1,1] neg_lo:[0,1,0]\n"
    "v_cvt_pk_bf16_f32 v83, v52, v54\n"
    "v_cvt_pk_bf16_f32 v87, v53, v55\n"
    "ds_write_b128 v16, v[80:83] offset:1152\n"
    "ds_write_b128 v16, v[84:87] offset:3456\n"
    "ds_read_b64_tr_b16 v[100:101], v17 offset:0\n"
    "ds_read_b64_tr_b16 v[102:103], v17 offset:2304\n"
    "ds_read_b64_tr_b16 v[104:105], v17 offset:256\n"
    "ds_read_b64_tr_b16 v[106:107], v17 offset:2560\n"
    "ds_read_b64_tr_b16 v[108:109], v17 offset:512\n"
    "ds_read_b64_tr_b16 v[110:111], v17 offset:2816\n"
    "ds_read_b64_tr_b16 v[112:113], v17 offset:768\n"
    "ds_read_b64_tr_b16 v[114:115], v17 offset:3072\n"
    "s_waitcnt vmcnt(3)\n"
    "v_lshlrev_b32 v10, 16, v10\n"
    "s_waitcnt vmcnt(2)\n"
    "v_lshlrev_b32 v11, 16, v11\n"
    "s_waitcnt vmcnt(1)\n"
    "v_lshlrev_b32 v12, 16, v12\n"
    "s_waitcnt vmcnt(0)\n"
    "v_lshlrev_b32 v13, 16, v13\n"
    "v_pk_mul_f32 v[10:11], v[10:11], v[14:15] op_sel:[0,1]\n"
    "v_pk_mul_f32 v[56:57], v[10:11], v[12:13] op_sel_hi:[1,0] neg_hi:[0,1]\n"
    "v_pk_fma_f32 v[56:57], v[10:11], v[12:13], v[56:57] op_sel:[1,1,0] op_sel_hi:[0,1,1] neg_lo:[0,1,0] neg_hi:[0,1,0]\n"
    "v_pk_mul_f32 v[58:59], v[56:57], v[44:45] op_sel_hi:[1,0]\n"
    "v_pk_fma_f32 v[58:59], v[56:57], v[44:45], v[58:59] op_sel:[1,1,0] op_sel_hi:[0,1,1] neg_hi:[0,1,0]\n"
    "v_pk_mul_f32 v[60:61], v[58:59], v[44:45] op_sel_hi:[1,0]\n"
    "v_pk_fma_f32 v[60:61], v[58:59], v[44:45], v[60:61] op_sel:[1,1,0] op_sel_hi:[0,1,1] neg_hi:[0,1,0]\n"
    "v_pk_mul_f32 v[62:63], v[60:61], v[44:45] op_sel_hi:[1,0]\n"
    "v_pk_fma_f32 v[62:63], v[60:61], v[44:45], v[62:63] op_sel:[1,1,0] op_sel_hi:[0,1,1] neg_hi:[0,1,0]\n"
    "v_pk_mul_f32 v[64:65], v[62:63], v[44:45] op_sel_hi:[1,0]\n"
    "v_pk_fma_f32 v[64:65], v[62:63], v[44:45], v[64:65] op_sel:[1,1,0] op_sel_hi:[0,1,1] neg_hi:[0,1,0]\n"
    "v_pk_mul_f32 v[66:67], v[64:65], v[44:45] op_sel_hi:[1,0]\n"
    "v_pk_fma_f32 v[66:67], v[64:65], v[44:45], v[66:67] op_sel:[1,1,0] op_sel_hi:[0,1,1] neg_hi:[0,1,0]\n"
    "v_pk_mul_f32 v[68:69], v[66:67], v[44:45] op_sel_hi:[1,0]\n"
    "v_pk_fma_f32 v[68:69], v[66:67], v[44:45], v[68:69] op_sel:[1,1,0] op_sel_hi:[0,1,1] neg_hi:[0,1,0]\n"
    "v_pk_mul_f32 v[70:71], v[68:69], v[44:45] op_sel_hi:[1,0]\n"
    "v_pk_fma_f32 v[70:71], v[68:69], v[44:45], v[70:71] op_sel:[1,1,0] op_sel_hi:[0,1,1] neg_hi:[0,1,0]\n"
    "v_pk_mul_f32 v[52:53], v[70:71], v[44:45] op_sel_hi:[1,0]\n"
    "v_pk_fma_f32 v[52:53], v[70:71], v[44:45], v[52:53] op_sel:[1,1,0] op_sel_hi:[0,1,1] neg_hi:[0,1,0]\n"
    "v_pk_mul_f32 v[54:55], v[52:53], v[44:45] op_sel_hi:[1,0]\n"
    "v_pk_fma_f32 v[54:55], v[52:53], v[44:45], v[54:55] op_sel:[1,1,0] op_sel_hi:[0,1,1] neg_hi:[0,1,0]\n"
    "v_cvt_pk_bf16_f32 v72, v52, v54\n"
    "v_cvt_pk_bf16_f32 v76, v53, v55\n"
    "v_pk_mul_f32 v[52:53], v[54:55], v[44:45] op_sel_hi:[1,0]\n"
    "v_pk_fma_f32 v[52:53], v[54:55], v[44:45], v[52:53] op_sel:[1,1,0] op_sel_hi:[0,1,1] neg_hi:[0,1,0]\n"
    "v_pk_mul_f32 v[54:55], v[52:53], v[44:45] op_sel_hi:[1,0]\n"
    "v_pk_fma_f32 v[54:55], v[52:53], v[44:45], v[54:55] op_sel:[1,1,0] op_sel_hi:[0,1,1] neg_hi:[0,1,0]\n"
    "v_cvt_pk_bf16_f32 v73, v52, v54\n"
    "v_cvt_pk_bf16_f32 v77, v53, v55\n"
    "v_pk_mul_f32 v[52:53], v[54:55], v[44:45] op_sel_hi:[1,0]\n"
    "v_pk_fma_f32 v[52:53], v[54:55], v[44:45], v[52:53] op_sel:[1,1,0] op_sel_hi:[0,1,1] neg_hi:[0,1,0]\n"
    "v_pk_mul_f32 v[54:55], v[52:53], v[44:45] op_sel_hi:[1,0]\n"
    "v_pk_fma_f32 v[54:55], v[52:53], v[44:45], v[54:55] op_sel:[1,1,0] op_sel_hi:[0,1,1] neg_hi:[0,1,0]\n"
    "v_cvt_pk_bf16_f32 v74, v52, v54\n"
    "v_cvt_pk_bf16_f32 v78, v53, v55\n"
    "v_pk_mul_f32 v[52:53], v[54:55], v[44:45] op_sel_hi:[1,0]\n"
    "v_pk_fma_f32 v[52:53], v[54:55], v[44:45], v[52:53] op_sel:[1,1,0] op_sel_hi:[0,1,1] neg_hi:[0,1,0]\n"
    "v_pk_mul_f32 v[54:55], v[52:53], v[44:45] op_sel_hi:[1,0]\n"
    "v_pk_fma_f32 v[54:55], v[52:53], v[44:45], v[54:55] op_sel:[1,1,0] op_sel_hi:[0,1,1] neg_hi:[0,1,0]\n"
    "v_cvt_pk_bf16_f32 v75, v52, v54\n"
    "v_cvt_pk_bf16_f32 v79, v53, v55\n"
    "ds_write_b128 v16, v[72:75] offset:0\n"
    "ds_write_b128 v16, v[76:79] offset:2304\n"
    "v_pk_mul_f32 v[52:53], v[54:55], v[44:45] op_sel_hi:[1,0]\n"
    "v_pk_fma_f32 v[52:53], v[54:55], v[44:45], v[52:53] op_sel:[1,1,0] op_sel_hi:[0,1,1] neg_hi:[0,1,0]\n"
    "v_cvt_pk_bf16_f32 v52, v52, v52\n"
    "ds_write_b32 v16, v52 offset:1152\n"
    "ds_read_b64_tr_b16 v[116:117], v17 offset:0\n"
    "ds_read_b64_tr_b16 v[118:119], v17 offset:2304\n"
    "ds_read_b64_tr_b16 v[120:121], v17 offset:256\n"
    "ds_read_b64_tr_b16 v[122:123], v17 offset:2560\n"
    "ds_read_b64_tr_b16 v[124:125], v17 offset:512\n"
    "ds_read_b64_tr_b16 v[126:127], v17 offset:2816\n"
    "ds_read_b64_tr_b16 v[128:129], v17 offset:768\n"
    "ds_read_b64_tr_b16 v[130:131], v17 offset:3072\n"
    "v_pk_mul_f32 v[54:55], v[56:57], v[40:41] op_sel_hi:[1,0]\n"
    "v_pk_fma_f32 v[54:55], v[56:57], v[40:41], v[54:55] op_sel:[1,1,0] op_sel_hi:[0,1,1] neg_hi:[0,1,0]\n"
    "v_cvt_pk_bf16_f32 v80, v56, v54\n"
    "v_cvt_pk_bf16_f32 v84, v57, v55\n"
    "v_pk_mul_f32 v[52:53], v[54:55], v[40:41] op_sel_hi:[1,0]\n"
    "v_pk_fma_f32 v[52:53], v[54:55], v[40:41], v[52:53] op_sel:[1,1,0] op_sel_hi:[0,1,1] neg_hi:[0,1,0]\n"
    "v_pk_mul_f32 v[54:55], v[52:53], v[40:41] op_sel_hi:[1,0]\n"
    "v_pk_fma_f32 v[54:55], v[52:53], v[40:41], v[54:55] op_sel:[1,1,0] op_sel_hi:[0,1,1] neg_hi:[0,1,0]\n"
    "v_cvt_pk_bf16_f32 v81, v52, v54\n"
    "v_cvt_pk_bf16_f32 v85, v53, v55\n"
    "v_pk_mul_f32 v[52:53], v[54:55], v[40:41] op_sel_hi:[1,0]\n"
    "v_pk_fma_f32 v[52:53], v[54:55], v[40:41], v[52:53] op_sel:[1,1,0] op_sel_hi:[0,1,1] neg_hi:[0,1,0]\n"
    "v_pk_mul_f32 v[54:55], v[52:53], v[40:41] op_sel_hi:[1,0]\n"
    "v_pk_fma_f32 v[54:55], v[52:53], v[40:41], v[54:55] op_sel:[1,1,0] op_sel_hi:[0,1,1] neg_hi:[0,1,0]\n"
    "v_cvt_pk_bf16_f32 v82, v52, v54\n"
    "v_cvt_pk_bf16_f32 v86, v53, v55\n"
    "v_pk_mul_f32 v[52:53], v[54:55], v[40:41] op_sel_hi:[1,0]\n"
    "v_pk_fma_f32 v[52:53], v[54:55], v[40:41], v[52:53] op_sel:[1,1,0] op_sel_hi:[0,1,1] neg_hi:[0,1,0]\n"
    "v_pk_mul_f32 v[54:55], v[52:53], v[40:41] op_sel_hi:[1,0]\n"
    "v_pk_fma_f32 v[54:55], v[52:53], v[40:41], v[54:55] op_sel:[1,1,0] op_sel_hi:[0,1,1] neg_hi:[0,1,0]\n"
    "v_cvt_pk_bf16_f32 v83, v52, v54\n"
    "v_cvt_pk_bf16_f32 v87, v53, v55\n"
    "s_waitcnt lgkmcnt(6)\n"
    "v_mfma_f32_16x16x32_bf16 v[96:99], v[100:103], v[116:119], 0\n"
    "ds_write_b128 v16, v[80:83] offset:0\n"
    "s_waitcnt lgkmcnt(5)\n"
    "v_mfma_f32_16x16x32_bf16 v[96:99], v[104:107], v[120:123], v[96:99]\n"
    "ds_write_b128 v16, v[84:87] offset:2304\n"
    "v_pk_mul_f32 v[54:55], v[58:59], v[40:41] op_sel_hi:[1,0]\n"
    "v_pk_fma_f32 v[54:55], v[58:59], v[40:41], v[54:55] op_sel:[1,1,0] op_sel_hi:[0,1,1] neg_hi:[0,1,0]\n"
    "v_cvt_pk_bf16_f32 v72, v58, v54\n"
    "v_cvt_pk_bf16_f32 v76, v59, v55\n"
    "v_pk_mul_f32 v[52:53], v[54:55], v[40:41] op_sel_hi:[1,0]\n"
    "v_pk_fma_f32 v[52:53], v[54:55], v[40:41], v[52:53] op_sel:[1,1,0] op_sel_hi:[0,1,1] neg_hi:[0,1,0]\n"
    "v_pk_mul_f32 v[54:55], v[52:53], v[40:41] op_sel_hi:[1,0]\n"
    "v_pk_fma_f32 v[54:55], v[52:53], v[40:41], v[54:55] op_sel:[1,1,0] op_sel_hi:[0,1,1] neg_hi:[0,1,0]\n"
    "v_cvt_pk_bf16_f32 v73, v52, v54\n"
    "v_cvt_pk_bf16_f32 v77, v53, v55\n"
    "v_pk_mul_f32 v[52:53], v[54:55], v[40:41] op_sel_hi:[1,0]\n"
    "v_pk_fma_f32 v[52:53], v[54:55], v[40:41], v[52:53] op_sel:[1,1,0] op_sel_hi:[0,1,1] neg_hi:[0,1,0]\n"
    "v_pk_mul_f32 v[54:55], v[52:53], v[40:41] op_sel_hi:[1,0]\n"
    "v_pk_fma_f32 v[54:55], v[52:53], v[40:41], v[54:55] op_sel:[1,1,0] op_sel_hi:[0,1,1] neg_hi:[0,1,0]\n"
    "v_cvt_pk_bf16_f32 v74, v52, v54\n"
    "v_cvt_pk_bf16_f32 v78, v53, v55\n"
    "v_pk_mul_f32 v[52:53], v[54:55], v[40:41] op_sel_hi:[1,0]\n"
    "v_pk_fma_f32 v[52:53], v[54:55], v[40:41], v[52:53] op_sel:[1,1,0] op_sel_hi:[0,1,1] neg_hi:[0,1,0]\n"
    "v_pk_mul_f32 v[54:55], v[52:53], v[40:41] op_sel_hi:[1,0]\n"
    "v_pk_fma_f32 v[54:55], v[52:53], v[40:41], v[54:55] op_sel:[1,1,0] op_sel_hi:[0,1,1] neg_hi:[0,1,0]\n"
    "v_cvt_pk_bf16_f32 v75, v52, v54\n"
    "v_cvt_pk_bf16_f32 v79, v53, v55\n"
    "s_waitcnt lgkmcnt(4)\n"
    "v_mfma_f32_16x16x32_bf16 v[96:99], v[108:111], v[124:127], v[96:99]\n"
    "ds_write_b128 v16, v[72:75] offset:1152\n"
    "s_waitcnt lgkmcnt(3)\n"
    "v_mfma_f32_16x16x32_bf16 v[96:99], v[112:115], v[128:131], v[96:99]\n"
    "ds_write_b128 v16, v[76:79] offset:3456\n"
    "ds_read_b64_tr_b16 v[132:133], v17 offset:0\n"
    "ds_read_b64_tr_b16 v[134:135], v17 offset:2304\n"
    "ds_read_b64_tr_b16 v[136:137], v17 offset:256\n"
    "ds_read_b64_tr_b16 v[138:139], v17 offset:2560\n"
    "ds_read_b64_tr_b16 v[140:141], v17 offset:512\n"
    "ds_read_b64_tr_b16 v[142:143], v17 offset:2816\n"
    "ds_read_b64_tr_b16 v[144:145], v17 offset:768\n"
    "ds_read_b64_tr_b16 v[146:147], v17 offset:3072\n"
    "v_pk_mul_f32 v[46:47], v[40:41], v[40:41] op_sel_hi:[1,0]\n"
    "v_pk_fma_f32 v[46:47], v[40:41], v[40:41], v[46:47] op_sel:[1,1,0] op_sel_hi:[0,1,1] neg_lo:[0,1,0]\n"
    "v_pk_mul_f32 v[48:49], v[46:47], v[46:47] op_sel_hi:[1,0]\n"
    "v_pk_fma_f32 v[48:49], v[46:47], v[46:47], v[48:49] op_sel:[1,1,0] op_sel_hi:[0,1,1] neg_lo:[0,1,0]\n"
    "v_pk_mul_f32 v[54:55], v[60:61], v[46:47] op_sel_hi:[1,0]\n"
    "v_pk_fma_f32 v[54:55], v[60:61], v[46:47], v[54:55] op_sel:[1,1,0] op_sel_hi:[0,1,1] neg_hi:[0,1,0]\n"
    "v_cvt_pk_bf16_f32 v80, v60, v54\n"
    "v_cvt_pk_bf16_f32 v84, v61, v55\n"
    "v_pk_mul_f32 v[52:53], v[54:55], v[46:47] op_sel_hi:[1,0]\n"
    "v_pk_fma_f32 v[52:53], v[54:55], v[46:47], v[52:53] op_sel:[1,1,0] op_sel_hi:[0,1,1] neg_hi:[0,1,0]\n"
    "v_pk_mul_f32 v[54:55], v[52:53], v[46:47] op_sel_hi:[1,0]\n"
    "v_pk_fma_f32 v[54:55], v[52:53], v[46:47], v[54:55] op_sel:[1,1,0] op_sel_hi:[0,1,1] neg_hi:[0,1,0]\n"
    "v_cvt_pk_bf16_f32 v81, v52, v54\n"
    "v_cvt_pk_bf16_f32 v85, v53, v55\n"
    "v_pk_mul_f32 v[54:55], v[62:63], v[46:47] op_sel_hi:[1,0]\n"
    "v_pk_fma_f32 v[54:55], v[62:63], v[46:47], v[54:55] op_sel:[1,1,0] op_sel_hi:[0,1,1] neg_hi:[0,1,0]\n"
    "v_cvt_pk_bf16_f32 v82, v62, v54\n"
    "v_cvt_pk_bf16_f32 v86, v63, v55\n"
    "v_pk_mul_f32 v[52:53], v[54:55], v[46:47] op_sel_hi:[1,0]\n"
    "v_pk_fma_f32 v[52:53], v[54:55], v[46:47], v[52:53] op_sel:[1,1,0] op_sel_hi:[0,1,1] neg_hi:[0,1,0]\n"
    "v_pk_mul_f32 v[54:55], v[52:53], v[46:47] op_sel_hi:[1,0]\n"
    "v_pk_fma_f32 v[54:55], v[52:53], v[46:47], v[54:55] op_sel:[1,1,0] op_sel_hi:[0,1,1] neg_hi:[0,1,0]\n"
    "v_cvt_pk_bf16_f32 v83, v52, v54\n"
    "v_cvt_pk_bf16_f32 v87, v53, v55\n"
    "s_waitcnt lgkmcnt(6)\n"
    "v_mfma_f32_16x16x32_bf16 v[88:91], v[132:135], v[100:103], v[88:91]\n"
    "ds_write_b128 v16, v[80:83] offset:0\n"
    "s_waitcnt lgkmcnt(5)\n"
    "v_mfma_f32_16x16x32_bf16 v[88:91], v[136:139], v[104:107], v[88:91]\n"
    "ds_write_b128 v16, v[84:87] offset:2304\n"
    "v_pk_mul_f32 v[54:55], v[64:65], v[48:49] op_sel_hi:[1,0]\n"
    "v_pk_fma_f32 v[54:55], v[64:65], v[48:49], v[54:55] op_sel:[1,1,0] op_sel_hi:[0,1,1] neg_hi:[0,1,0]\n"
    "v_cvt_pk_bf16_f32 v72, v64, v54\n"
    "v_cvt_pk_bf16_f32 v76, v65, v55\n"
    "v_pk_mul_f32 v[54:55], v[66:67], v[48:49] op_sel_hi:[1,0]\n"
    "v_pk_fma_f32 v[54:55], v[66:67], v[48:49], v[54:55] op_sel:[1,1,0] op_sel_hi:[0,1,1] neg_hi:[0,1,0]\n"
    "v_cvt_pk_bf16_f32 v73, v66, v54\n"
    "v_cvt_pk_bf16_f32 v77, v67, v55\n"
    "v_pk_mul_f32 v[54:55], v[68:69], v[48:49] op_sel_hi:[1,0]\n"
    "v_pk_fma_f32 v[54:55], v[68:69], v[48:49], v[54:55] op_sel:[1,1,0] op_sel_hi:[0,1,1] neg_hi:[0,1,0]\n"
    "v_cvt_pk_bf16_f32 v74, v68, v54\n"
    "v_cvt_pk_bf16_f32 v78, v69, v55\n"
    "v_pk_mul_f32 v[54:55], v[70:71], v[48:49] op_sel_hi:[1,0]\n"
    "v_pk_fma_f32 v[54:55], v[70:71], v[48:49], v[54:55] op_sel:[1,1,0] op_sel_hi:[0,1,1] neg_hi:[0,1,0]\n"
    "v_cvt_pk_bf16_f32 v75, v70, v54\n"
    "v_cvt_pk_bf16_f32 v79, v71, v55\n"
    "s_waitcnt lgkmcnt(4)\n"
    "v_mfma_f32_16x16x32_bf16 v[88:91], v[140:143], v[108:111], v[88:91]\n"
    "ds_write_b128 v16, v[72:75] offset:1152\n"
    "s_waitcnt lgkmcnt(3)\n"
    "v_mfma_f32_16x16x32_bf16 v[88:91], v[144:147], v[112:115], v[88:91]\n"
    "ds_write_b128 v16, v[76:79] offset:3456\n"
    "ds_read_b64_tr_b16 v[116:117], v17 offset:0\n"
    "ds_read_b64_tr_b16 v[118:119], v17 offset:2304\n"
    "ds_read_b64_tr_b16 v[120:121], v17 offset:256\n"
    "ds_read_b64_tr_b16 v[122:123], v17 offset:2560\n"
    "ds_read_b64_tr_b16 v[124:125], v17 offset:512\n"
    "ds_read_b64_tr_b16 v[126:127], v17 offset:2816\n"
    "ds_read_b64_tr_b16 v[128:129], v17 offset:768\n"
    "ds_read_b64_tr_b16 v[130:131], v17 offset:3072\n"
    "ds_write_b128 v21, v[96:99]\n"
    "ds_write_b128 v18, v[88:91]\n"
    "s_waitcnt lgkmcnt(8)\n"
    "v_mfma_f32_16x16x32_bf16 v[92:95], v[116:119], v[100:103], 0\n"
    "s_waitcnt lgkmcnt(6)\n"
    "v_mfma_f32_16x16x32_bf16 v[92:95], v[120:123], v[104:107], v[92:95]\n"
    "s_waitcnt lgkmcnt(4)\n"
    "v_mfma_f32_16x16x32_bf16 v[92:95], v[124:127], v[108:111], v[92:95]\n"
    "s_waitcnt lgkmcnt(2)\n"
    "v_mfma_f32_16x16x32_bf16 v[92:95], v[128:131], v[112:115], v[92:95]\n"
    "s_nop 7\n"
    "ds_write_b64 v19, v[92:93]\n"
    "ds_write_b64 v20, v[94:95]\n"
    "s_waitcnt lgkmcnt(0)\n"
    "s_setprio 0\n"
    "s_barrier\n"
    "ds_read_b128 v[96:99], v224 offset:0\n"
    "ds_read_b64_tr_b16 v[112:113], v228 offset:0\n"
    "ds_read_b64_tr_b16 v[114:115], v228 offset:768\n"
    "ds_read_b128 v[100:103], v224 offset:2048\n"
    "ds_read_b128 v[104:107], v224 offset:4096\n"
    "ds_read_b128 v[108:111], v224 offset:6144\n"
    "ds_read_b64_tr_b16 v[116:117], v228 offset:12288\n"
    "ds_read_b64_tr_b16 v[118:119], v228 offset:13056\n"
    "ds_read_b64_tr_b16 v[120:121], v229 offset:32\n"
    "ds_read_b64_tr_b16 v[122:123], v229 offset:800\n"
    "ds_read_b64_tr_b16 v[124:125], v229 offset:12320\n"
    "ds_read_b64_tr_b16 v[126:127], v229 offset:13088\n"
    "ds_read_b64_tr_b16 v[128:129], v228 offset:64\n"
    "ds_read_b64_tr_b16 v[130:131], v228 offset:832\n"
    "ds_read_b64_tr_b16 v[132:133], v228 offset:12352\n"
    "ds_read_b64_tr_b16 v[134:135], v228 offset:13120\n"
    "s_waitcnt lgkmcnt(13)\n"
    "v_mfma_f32_16x16x32_bf16 v[0:3], v[112:115], v[96:99], 0\n"
    "ds_read_b128 v[136:139], v225 offset:0\n"
    "s_waitcnt lgkmcnt(13)\n"
    "v_mfma_f32_16x16x32_bf16 v[4:7], v[112:115], v[100:103], 0\n"
    "ds_read_b64_tr_b16 v[152:153], v228 offset:6144\n"
    "s_waitcnt lgkmcnt(13)\n"
    "v_mfma_f32_16x16x32_bf16 v[48:51], v[112:115], v[104:107], 0\n"
    "ds_read_b64_tr_b16 v[154:155], v228 offset:6912\n"
    "s_waitcnt lgkmcnt(13)\n"
    "v_mfma_f32_16x16x32_bf16 v[52:55], v[112:115], v[108:111], 0\n"
    "ds_read_b128 v[140:143], v225 offset:2048\n"
    "s_waitcnt lgkmcnt(12)\n"
    "v_mfma_f32_16x16x32_bf16 v[8:11], v[116:119], v[96:99], 0\n"
    "ds_read_b128 v[144:147], v225 offset:4096\n"
    "v_mfma_f32_16x16x32_bf16 v[12:15], v[116:119], v[100:103], 0\n"
    "ds_read_b128 v[148:151], v225 offset:6144\n"
    "v_mfma_f32_16x16x32_bf16 v[56:59], v[116:119], v[104:107], 0\n"
    "ds_read_b64_tr_b16 v[156:157], v228 offset:18432\n"
    "v_mfma_f32_16x16x32_bf16 v[60:63], v[116:119], v[108:111], 0\n"
    "ds_read_b64_tr_b16 v[158:159], v228 offset:19200\n"
    "s_waitcnt lgkmcnt(14)\n"
    "v_mfma_f32_16x16x32_bf16 v[16:19], v[120:123], v[96:99], 0\n"
    "ds_read_b64_tr_b16 v[160:161], v229 offset:6176\n"
    "v_mfma_f32_16x16x32_bf16 v[20:23], v[120:123], v[100:103], 0\n"
    "ds_read_b64_tr_b16 v[162:163], v229 offset:6944\n"
    "v_mfma_f32_16x16x32_bf16 v[64:67], v[120:123], v[104:107], 0\n"
    "ds_read_b64_tr_b16 v[164:165], v229 offset:18464\n"
    "v_mfma_f32_16x16x32_bf16 v[68:71], v[120:123], v[108:111], 0\n"
    "ds_read_b64_tr_b16 v[166:167], v229 offset:19232\n"
    "s_waitcnt lgkmcnt(15)\n"
    "v_mfma_f32_16x16x32_bf16 v[24:27], v[124:127], v[96:99], 0\n"
    "ds_read_b64_tr_b16 v[168:169], v228 offset:6208\n"
    "v_mfma_f32_16x16x32_bf16 v[28:31], v[124:127], v[100:103], 0\n"
    "ds_read_b64_tr_b16 v[170:171], v228 offset:6976\n"
    "v_mfma_f32_16x16x32_bf16 v[72:75], v[124:127], v[104:107], 0\n"
    "ds_read_b64_tr_b16 v[172:173], v228 offset:18496\n"
    "v_mfma_f32_16x16x32_bf16 v[76:79], v[124:127], v[108:111], 0\n"
    "ds_read_b64_tr_b16 v[174:175], v228 offset:19264\n"
    "s_waitcnt lgkmcnt(15)\n"
    "v_mfma_f32_16x16x32_bf16 v[32:35], v[128:131], v[96:99], 0\n"
    "v_mfma_f32_16x16x32_bf16 v[36:39], v[128:131], v[100:103], 0\n"
    "v_mfma_f32_16x16x32_bf16 v[80:83], v[128:131], v[104:107], 0\n"
    "v_mfma_f32_16x16x32_bf16 v[84:87], v[128:131], v[108:111], 0\n"
    "v_mfma_f32_16x16x32_bf16 v[40:43], v[132:135], v[96:99], 0\n"
    "v_mfma_f32_16x16x32_bf16 v[44:47], v[132:135], v[100:103], 0\n"
    "v_mfma_f32_16x16x32_bf16 v[88:91], v[132:135], v[104:107], 0\n"
    "v_mfma_f32_16x16x32_bf16 v[92:95], v[132:135], v[108:111], 0\n"
    "s_waitcnt lgkmcnt(13)\n"
    "v_mfma_f32_16x16x32_bf16 v[0:3], v[152:155], v[136:139], v[0:3]\n"
    "s_waitcnt lgkmcnt(12)\n"
    "v_mfma_f32_16x16x32_bf16 v[4:7], v[152:155], v[140:143], v[4:7]\n"
    "s_waitcnt lgkmcnt(11)\n"
    "v_mfma_f32_16x16x32_bf16 v[48:51], v[152:155], v[144:147], v[48:51]\n"
    "s_waitcnt lgkmcnt(10)\n"
    "v_mfma_f32_16x16x32_bf16 v[52:55], v[152:155], v[148:151], v[52:55]\n"
    "s_waitcnt lgkmcnt(8)\n"
    "v_mfma_f32_16x16x32_bf16 v[8:11], v[156:159], v[136:139], v[8:11]\n"
    "v_mfma_f32_16x16x32_bf16 v[12:15], v[156:159], v[140:143], v[12:15]\n"
    "v_mfma_f32_16x16x32_bf16 v[56:59], v[156:159], v[144:147], v[56:59]\n"
    "v_mfma_f32_16x16x32_bf16 v[60:63], v[156:159], v[148:151], v[60:63]\n"
    "s_waitcnt lgkmcnt(0)\n"
    "s_barrier\n"
    "ds_read_b128 v[96:99], v224 offset:40960\n"
    "ds_read_b64_tr_b16 v[112:113], v228 offset:40960\n"
    "v_mfma_f32_16x16x32_bf16 v[16:19], v[160:163], v[136:139], v[16:19]\n"
    "ds_read_b64_tr_b16 v[114:115], v228 offset:41728\n"
    "v_mfma_f32_16x16x32_bf16 v[20:23], v[160:163], v[140:143], v[20:23]\n"
    "ds_read_b128 v[100:103], v224 offset:43008\n"
    "v_mfma_f32_16x16x32_bf16 v[64:67], v[160:163], v[144:147], v[64:67]\n"
    "ds_read_b128 v[104:107], v224 offset:45056\n"
    "v_mfma_f32_16x16x32_bf16 v[68:71], v[160:163], v[148:151], v[68:71]\n"
    "ds_read_b128 v[108:111], v224 offset:47104\n"
    "v_mfma_f32_16x16x32_bf16 v[24:27], v[164:167], v[136:139], v[24:27]\n"
    "ds_read_b64_tr_b16 v[116:117], v228 offset:53248\n"
    "v_mfma_f32_16x16x32_bf16 v[28:31], v[164:167], v[140:143], v[28:31]\n"
    "ds_read_b64_tr_b16 v[118:119], v228 offset:54016\n"
    "v_mfma_f32_16x16x32_bf16 v[72:75], v[164:167], v[144:147], v[72:75]\n"
    "ds_read_b64_tr_b16 v[120:121], v229 offset:40992\n"
    "v_mfma_f32_16x16x32_bf16 v[76:79], v[164:167], v[148:151], v[76:79]\n"
    "ds_read_b64_tr_b16 v[122:123], v229 offset:41760\n"
    "v_mfma_f32_16x16x32_bf16 v[32:35], v[168:171], v[136:139], v[32:35]\n"
    "ds_read_b64_tr_b16 v[124:125], v229 offset:53280\n"
    "v_mfma_f32_16x16x32_bf16 v[36:39], v[168:171], v[140:143], v[36:39]\n"
    "ds_read_b64_tr_b16 v[126:127], v229 offset:54048\n"
    "v_mfma_f32_16x16x32_bf16 v[80:83], v[168:171], v[144:147], v[80:83]\n"
    "ds_read_b64_tr_b16 v[128:129], v228 offset:41024\n"
    "v_mfma_f32_16x16x32_bf16 v[84:87], v[168:171], v[148:151], v[84:87]\n"
    "ds_read_b64_tr_b16 v[130:131], v228 offset:41792\n"
    "v_mfma_f32_16x16x32_bf16 v[40:43], v[172:175], v[136:139], v[40:43]\n"
    "ds_read_b64_tr_b16 v[132:133], v228 offset:53312\n"
    "v_mfma_f32_16x16x32_bf16 v[44:47], v[172:175], v[140:143], v[44:47]\n"
    "ds_read_b64_tr_b16 v[134:135], v228 offset:54080\n"
    "v_mfma_f32_16x16x32_bf16 v[88:91], v[172:175], v[144:147], v[88:91]\n"
    "v_mfma_f32_16x16x32_bf16 v[92:95], v[172:175], v[148:151], v[92:95]\n"
    "s_waitcnt lgkmcnt(13)\n"
    "v_mfma_f32_16x16x32_bf16 v[0:3], v[112:115], v[96:99], v[0:3]\n"
    "ds_read_b128 v[136:139], v225 offset:40960\n"
    "s_waitcnt lgkmcnt(13)\n"
    "v_mfma_f32_16x16x32_bf16 v[4:7], v[112:115], v[100:103], v[4:7]\n"
    "ds_read_b64_tr_b16 v[152:153], v228 offset:47104\n"
    "s_waitcnt lgkmcnt(13)\n"
    "v_mfma_f32_16x16x32_bf16 v[48:51], v[112:115], v[104:107], v[48:51]\n"
    "ds_read_b64_tr_b16 v[154:155], v228 offset:47872\n"
    "s_waitcnt lgkmcnt(13)\n"
    "v_mfma_f32_16x16x32_bf16 v[52:55], v[112:115], v[108:111], v[52:55]\n"
    "ds_read_b128 v[140:143], v225 offset:43008\n"
    "s_waitcnt lgkmcnt(12)\n"
    "v_mfma_f32_16x16x32_bf16 v[8:11], v[116:119], v[96:99], v[8:11]\n"
    "ds_read_b128 v[144:147], v225 offset:45056\n"
    "v_mfma_f32_16x16x32_bf16 v[12:15], v[116:119], v[100:103], v[12:15]\n"
    "ds_read_b128 v[148:151], v225 offset:47104\n"
    "v_mfma_f32_16x16x32_bf16 v[56:59], v[116:119], v[104:107], v[56:59]\n"
    "ds_read_b64_tr_b16 v[156:157], v228 offset:59392\n"
    "v_mfma_f32_16x16x32_bf16 v[60:63], v[116:119], v[108:111], v[60:63]\n"
    "ds_read_b64_tr_b16 v[158:159], v228 offset:60160\n"
    "s_waitcnt lgkmcnt(14)\n"
    "v_mfma_f32_16x16x32_bf16 v[16:19], v[120:123], v[96:99], v[16:19]\n"
    "ds_read_b64_tr_b16 v[160:161], v229 offset:47136\n"
    "v_mfma_f32_16x16x32_bf16 v[20:23], v[120:123], v[100:103], v[20:23]\n"
    "ds_read_b64_tr_b16 v[162:163], v229 offset:47904\n"
    "v_mfma_f32_16x16x32_bf16 v[64:67], v[120:123], v[104:107], v[64:67]\n"
    "ds_read_b64_tr_b16 v[164:165], v229 offset:59424\n"
    "v_mfma_f32_16x16x32_bf16 v[68:71], v[120:123], v[108:111], v[68:71]\n"
    "ds_read_b64_tr_b16 v[166:167], v229 offset:60192\n"
    "s_waitcnt lgkmcnt(15)\n"
    "v_mfma_f32_16x16x32_bf16 v[24:27], v[124:127], v[96:99], v[24:27]\n"
    "ds_read_b64_tr_b16 v[168:169], v228 offset:47168\n"
    "v_mfma_f32_16x16x32_bf16 v[28:31], v[124:127], v[100:103], v[28:31]\n"
    "ds_read_b64_tr_b16 v[170:171], v228 offset:47936\n"
    "v_mfma_f32_16x16x32_bf16 v[72:75], v[124:127], v[104:107], v[72:75]\n"
    "ds_read_b64_tr_b16 v[172:173], v228 offset:59456\n"
    "v_mfma_f32_16x16x32_bf16 v[76:79], v[124:127], v[108:111], v[76:79]\n"
    "ds_read_b64_tr_b16 v[174:175], v228 offset:60224\n"
    "s_waitcnt lgkmcnt(15)\n"
    "v_mfma_f32_16x16x32_bf16 v[32:35], v[128:131], v[96:99], v[32:35]\n"
    "v_mfma_f32_16x16x32_bf16 v[36:39], v[128:131], v[100:103], v[36:39]\n"
    "v_mfma_f32_16x16x32_bf16 v[80:83], v[128:131], v[104:107], v[80:83]\n"
    "v_mfma_f32_16x16x32_bf16 v[84:87], v[128:131], v[108:111], v[84:87]\n"
    "v_mfma_f32_16x16x32_bf16 v[40:43], v[132:135], v[96:99], v[40:43]\n"
    "v_mfma_f32_16x16x32_bf16 v[44:47], v[132:135], v[100:103], v[44:47]\n"
    "v_mfma_f32_16x16x32_bf16 v[88:91], v[132:135], v[104:107], v[88:91]\n"
    "v_mfma_f32_16x16x32_bf16 v[92:95], v[132:135], v[108:111], v[92:95]\n"
    "s_waitcnt lgkmcnt(13)\n"
    "v_mfma_f32_16x16x32_bf16 v[0:3], v[152:155], v[136:139], v[0:3]\n"
    "s_waitcnt lgkmcnt(12)\n"
    "v_mfma_f32_16x16x32_bf16 v[4:7], v[152:155], v[140:143], v[4:7]\n"
    "s_waitcnt lgkmcnt(11)\n"
    "v_mfma_f32_16x16x32_bf16 v[48:51], v[152:155], v[144:147], v[48:51]\n"
    "s_waitcnt lgkmcnt(10)\n"
    "v_mfma_f32_16x16x32_bf16 v[52:55], v[152:155], v[148:151], v[52:55]\n"
    "s_waitcnt lgkmcnt(8)\n"
    "v_mfma_f32_16x16x32_bf16 v[8:11], v[156:159], v[136:139], v[8:11]\n"
    "v_mfma_f32_16x16x32_bf16 v[12:15], v[156:159], v[140:143], v[12:15]\n"
    "v_mfma_f32_16x16x32_bf16 v[56:59], v[156:159], v[144:147], v[56:59]\n"
    "v_mfma_f32_16x16x32_bf16 v[60:63], v[156:159], v[148:151], v[60:63]\n"
    "s_waitcnt lgkmcnt(0)\n"
    "s_barrier\n"
    "ds_read_b128 v[96:99], v226 offset:0\n"
    "ds_read_b64_tr_b16 v[112:113], v230 offset:0\n"
    "v_mfma_f32_16x16x32_bf16 v[16:19], v[160:163], v[136:139], v[16:19]\n"
    "ds_read_b64_tr_b16 v[114:115], v230 offset:768\n"
    "v_mfma_f32_16x16x32_bf16 v[20:23], v[160:163], v[140:143], v[20:23]\n"
    "ds_read_b128 v[100:103], v226 offset:2048\n"
    "v_mfma_f32_16x16x32_bf16 v[64:67], v[160:163], v[144:147], v[64:67]\n"
    "ds_read_b128 v[104:107], v226 offset:4096\n"
    "v_mfma_f32_16x16x32_bf16 v[68:71], v[160:163], v[148:151], v[68:71]\n"
    "ds_read_b128 v[108:111], v226 offset:6144\n"
    "v_mfma_f32_16x16x32_bf16 v[24:27], v[164:167], v[136:139], v[24:27]\n"
    "ds_read_b64_tr_b16 v[116:117], v230 offset:12288\n"
    "v_mfma_f32_16x16x32_bf16 v[28:31], v[164:167], v[140:143], v[28:31]\n"
    "ds_read_b64_tr_b16 v[118:119], v230 offset:13056\n"
    "v_mfma_f32_16x16x32_bf16 v[72:75], v[164:167], v[144:147], v[72:75]\n"
    "ds_read_b64_tr_b16 v[120:121], v231 offset:32\n"
    "v_mfma_f32_16x16x32_bf16 v[76:79], v[164:167], v[148:151], v[76:79]\n"
    "ds_read_b64_tr_b16 v[122:123], v231 offset:800\n"
    "v_mfma_f32_16x16x32_bf16 v[32:35], v[168:171], v[136:139], v[32:35]\n"
    "ds_read_b64_tr_b16 v[124:125], v231 offset:12320\n"
    "v_mfma_f32_16x16x32_bf16 v[36:39], v[168:171], v[140:143], v[36:39]\n"
    "ds_read_b64_tr_b16 v[126:127], v231 offset:13088\n"
    "v_mfma_f32_16x16x32_bf16 v[80:83], v[168:171], v[144:147], v[80:83]\n"
    "ds_read_b64_tr_b16 v[128:129], v230 offset:64\n"
    "v_mfma_f32_16x16x32_bf16 v[84:87], v[168:171], v[148:151], v[84:87]\n"
    "ds_read_b64_tr_b16 v[130:131], v230 offset:832\n"
    "v_mfma_f32_16x16x32_bf16 v[40:43], v[172:175], v[136:139], v[40:43]\n"
    "ds_read_b64_tr_b16 v[132:133], v230 offset:12352\n"
    "v_mfma_f32_16x16x32_bf16 v[44:47], v[172:175], v[140:143], v[44:47]\n"
    "ds_read_b64_tr_b16 v[134:135], v230 offset:13120\n"
    "v_mfma_f32_16x16x32_bf16 v[88:91], v[172:175], v[144:147], v[88:91]\n"
    "v_mfma_f32_16x16x32_bf16 v[92:95], v[172:175], v[148:151], v[92:95]\n"
    "s_waitcnt lgkmcnt(13)\n"
    "v_mfma_f32_16x16x32_bf16 v[0:3], v[112:115], v[96:99], v[0:3]\n"
    "ds_read_b128 v[136:139], v227 offset:0\n"
    "s_waitcnt lgkmcnt(13)\n"
    "v_mfma_f32_16x16x32_bf16 v[4:7], v[112:115], v[100:103], v[4:7]\n"
    "ds_read_b64_tr_b16 v[152:153], v230 offset:6144\n"
    "s_waitcnt lgkmcnt(13)\n"
    "v_mfma_f32_16x16x32_bf16 v[48:51], v[112:115], v[104:107], v[48:51]\n"
    "ds_read_b64_tr_b16 v[154:155], v230 offset:6912\n"
    "s_waitcnt lgkmcnt(13)\n"
    "v_mfma_f32_16x16x32_bf16 v[52:55], v[112:115], v[108:111], v[52:55]\n"
    "ds_read_b128 v[140:143], v227 offset:2048\n"
    "s_waitcnt lgkmcnt(12)\n"
    "v_mfma_f32_16x16x32_bf16 v[8:11], v[116:119], v[96:99], v[8:11]\n"
    "ds_read_b128 v[144:147], v227 offset:4096\n"
    "v_mfma_f32_16x16x32_bf16 v[12:15], v[116:119], v[100:103], v[12:15]\n"
    "ds_read_b128 v[148:151], v227 offset:6144\n"
    "v_mfma_f32_16x16x32_bf16 v[56:59], v[116:119], v[104:107], v[56:59]\n"
    "ds_read_b64_tr_b16 v[156:157], v230 offset:18432\n"
    "v_mfma_f32_16x16x32_bf16 v[60:63], v[116:119], v[108:111], v[60:63]\n"
    "ds_read_b64_tr_b16 v[158:159], v230 offset:19200\n"
    "s_waitcnt lgkmcnt(14)\n"
    "v_mfma_f32_16x16x32_bf16 v[16:19], v[120:123], v[96:99], v[16:19]\n"
    "ds_read_b64_tr_b16 v[160:161], v231 offset:6176\n"
    "v_mfma_f32_16x16x32_bf16 v[20:23], v[120:123], v[100:103], v[20:23]\n"
    "ds_read_b64_tr_b16 v[162:163], v231 offset:6944\n"
    "v_mfma_f32_16x16x32_bf16 v[64:67], v[120:123], v[104:107], v[64:67]\n"
    "ds_read_b64_tr_b16 v[164:165], v231 offset:18464\n"
    "v_mfma_f32_16x16x32_bf16 v[68:71], v[120:123], v[108:111], v[68:71]\n"
    "ds_read_b64_tr_b16 v[166:167], v231 offset:19232\n"
    "s_waitcnt lgkmcnt(15)\n"
    "v_mfma_f32_16x16x32_bf16 v[24:27], v[124:127], v[96:99], v[24:27]\n"
    "ds_read_b64_tr_b16 v[168:169], v230 offset:6208\n"
    "v_mfma_f32_16x16x32_bf16 v[28:31], v[124:127], v[100:103], v[28:31]\n"
    "ds_read_b64_tr_b16 v[170:171], v230 offset:6976\n"
    "v_mfma_f32_16x16x32_bf16 v[72:75], v[124:127], v[104:107], v[72:75]\n"
    "ds_read_b64_tr_b16 v[172:173], v230 offset:18496\n"
    "v_mfma_f32_16x16x32_bf16 v[76:79], v[124:127], v[108:111], v[76:79]\n"
    "ds_read_b64_tr_b16 v[174:175], v230 offset:19264\n"
    "s_waitcnt lgkmcnt(15)\n"
    "v_mfma_f32_16x16x32_bf16 v[32:35], v[128:131], v[96:99], v[32:35]\n"
    "v_mfma_f32_16x16x32_bf16 v[36:39], v[128:131], v[100:103], v[36:39]\n"
    "v_mfma_f32_16x16x32_bf16 v[80:83], v[128:131], v[104:107], v[80:83]\n"
    "v_mfma_f32_16x16x32_bf16 v[84:87], v[128:131], v[108:111], v[84:87]\n"
    "v_mfma_f32_16x16x32_bf16 v[40:43], v[132:135], v[96:99], v[40:43]\n"
    "v_mfma_f32_16x16x32_bf16 v[44:47], v[132:135], v[100:103], v[44:47]\n"
    "v_mfma_f32_16x16x32_bf16 v[88:91], v[132:135], v[104:107], v[88:91]\n"
    "v_mfma_f32_16x16x32_bf16 v[92:95], v[132:135], v[108:111], v[92:95]\n"
    "s_waitcnt lgkmcnt(13)\n"
    "v_mfma_f32_16x16x32_bf16 v[0:3], v[152:155], v[136:139], v[0:3]\n"
    "s_waitcnt lgkmcnt(12)\n"
    "v_mfma_f32_16x16x32_bf16 v[4:7], v[152:155], v[140:143], v[4:7]\n"
    "s_waitcnt lgkmcnt(11)\n"
    "v_mfma_f32_16x16x32_bf16 v[48:51], v[152:155], v[144:147], v[48:51]\n"
    "s_waitcnt lgkmcnt(10)\n"
    "v_mfma_f32_16x16x32_bf16 v[52:55], v[152:155], v[148:151], v[52:55]\n"
    "s_waitcnt lgkmcnt(8)\n"
    "v_mfma_f32_16x16x32_bf16 v[8:11], v[156:159], v[136:139], v[8:11]\n"
    "v_mfma_f32_16x16x32_bf16 v[12:15], v[156:159], v[140:143], v[12:15]\n"
    "v_mfma_f32_16x16x32_bf16 v[56:59], v[156:159], v[144:147], v[56:59]\n"
    "v_mfma_f32_16x16x32_bf16 v[60:63], v[156:159], v[148:151], v[60:63]\n"
    "s_waitcnt lgkmcnt(0)\n"
    "s_barrier\n"
    "ds_read_b128 v[96:99], v224 offset:0\n"
    "ds_read_b64_tr_b16 v[112:113], v228 offset:0\n"
    "v_mfma_f32_16x16x32_bf16 v[16:19], v[160:163], v[136:139], v[16:19]\n"
    "ds_read_b64_tr_b16 v[114:115], v228 offset:768\n"
    "v_mfma_f32_16x16x32_bf16 v[20:23], v[160:163], v[140:143], v[20:23]\n"
    "ds_read_b128 v[100:103], v224 offset:2048\n"
    "v_mfma_f32_16x16x32_bf16 v[64:67], v[160:163], v[144:147], v[64:67]\n"
    "ds_read_b128 v[104:107], v224 offset:4096\n"
    "v_mfma_f32_16x16x32_bf16 v[68:71], v[160:163], v[148:151], v[68:71]\n"
    "ds_read_b128 v[108:111], v224 offset:6144\n"
    "v_mfma_f32_16x16x32_bf16 v[24:27], v[164:167], v[136:139], v[24:27]\n"
    "ds_read_b64_tr_b16 v[116:117], v228 offset:12288\n"
    "v_mfma_f32_16x16x32_bf16 v[28:31], v[164:167], v[140:143], v[28:31]\n"
    "ds_read_b64_tr_b16 v[118:119], v228 offset:13056\n"
    "v_mfma_f32_16x16x32_bf16 v[72:75], v[164:167], v[144:147], v[72:75]\n"
    "ds_read_b64_tr_b16 v[120:121], v229 offset:32\n"
    "v_mfma_f32_16x16x32_bf16 v[76:79], v[164:167], v[148:151], v[76:79]\n"
    "ds_read_b64_tr_b16 v[122:123], v229 offset:800\n"
    "v_mfma_f32_16x16x32_bf16 v[32:35], v[168:171], v[136:139], v[32:35]\n"
    "ds_read_b64_tr_b16 v[124:125], v229 offset:12320\n"
    "v_mfma_f32_16x16x32_bf16 v[36:39], v[168:171], v[140:143], v[36:39]\n"
    "ds_read_b64_tr_b16 v[126:127], v229 offset:13088\n"
    "v_mfma_f32_16x16x32_bf16 v[80:83], v[168:171], v[144:147], v[80:83]\n"
    "ds_read_b64_tr_b16 v[128:129], v228 offset:64\n"
    "v_mfma_f32_16x16x32_bf16 v[84:87], v[168:171], v[148:151], v[84:87]\n"
    "ds_read_b64_tr_b16 v[130:131], v228 offset:832\n"
    "v_mfma_f32_16x16x32_bf16 v[40:43], v[172:175], v[136:139], v[40:43]\n"
    "ds_read_b64_tr_b16 v[132:133], v228 offset:12352\n"
    "v_mfma_f32_16x16x32_bf16 v[44:47], v[172:175], v[140:143], v[44:47]\n"
    "ds_read_b64_tr_b16 v[134:135], v228 offset:13120\n"
    "v_mfma_f32_16x16x32_bf16 v[88:91], v[172:175], v[144:147], v[88:91]\n"
    "v_mfma_f32_16x16x32_bf16 v[92:95], v[172:175], v[148:151], v[92:95]\n"
    "s_waitcnt lgkmcnt(13)\n"
    "v_mfma_f32_16x16x32_bf16 v[0:3], v[112:115], v[96:99], v[0:3]\n"
    "ds_read_b128 v[136:139], v225 offset:0\n"
    "s_waitcnt lgkmcnt(13)\n"
    "v_mfma_f32_16x16x32_bf16 v[4:7], v[112:115], v[100:103], v[4:7]\n"
    "ds_read_b64_tr_b16 v[152:153], v228 offset:6144\n"
    "s_waitcnt lgkmcnt(13)\n"
    "v_mfma_f32_16x16x32_bf16 v[48:51], v[112:115], v[104:107], v[48:51]\n"
    "ds_read_b64_tr_b16 v[154:155], v228 offset:6912\n"
    "s_waitcnt lgkmcnt(13)\n"
    "v_mfma_f32_16x16x32_bf16 v[52:55], v[112:115], v[108:111], v[52:55]\n"
    "ds_read_b128 v[140:143], v225 offset:2048\n"
    "s_waitcnt lgkmcnt(12)\n"
    "v_mfma_f32_16x16x32_bf16 v[8:11], v[116:119], v[96:99], v[8:11]\n"
    "ds_read_b128 v[144:147], v225 offset:4096\n"
    "v_mfma_f32_16x16x32_bf16 v[12:15], v[116:119], v[100:103], v[12:15]\n"
    "ds_read_b128 v[148:151], v225 offset:6144\n"
    "v_mfma_f32_16x16x32_bf16 v[56:59], v[116:119], v[104:107], v[56:59]\n"
    "ds_read_b64_tr_b16 v[156:157], v228 offset:18432\n"
    "v_mfma_f32_16x16x32_bf16 v[60:63], v[116:119], v[108:111], v[60:63]\n"
    "ds_read_b64_tr_b16 v[158:159], v228 offset:19200\n"
    "s_waitcnt lgkmcnt(14)\n"
    "v_mfma_f32_16x16x32_bf16 v[16:19], v[120:123], v[96:99], v[16:19]\n"
    "ds_read_b64_tr_b16 v[160:161], v229 offset:6176\n"
    "v_mfma_f32_16x16x32_bf16 v[20:23], v[120:123], v[100:103], v[20:23]\n"
    "ds_read_b64_tr_b16 v[162:163], v229 offset:6944\n"
    "v_mfma_f32_16x16x32_bf16 v[64:67], v[120:123], v[104:107], v[64:67]\n"
    "ds_read_b64_tr_b16 v[164:165], v229 offset:18464\n"
    "v_mfma_f32_16x16x32_bf16 v[68:71], v[120:123], v[108:111], v[68:71]\n"
    "ds_read_b64_tr_b16 v[166:167], v229 offset:19232\n"
    "s_waitcnt lgkmcnt(15)\n"
    "v_mfma_f32_16x16x32_bf16 v[24:27], v[124:127], v[96:99], v[24:27]\n"
    "ds_read_b64_tr_b16 v[168:169], v228 offset:6208\n"
    "v_mfma_f32_16x16x32_bf16 v[28:31], v[124:127], v[100:103], v[28:31]\n"
    "ds_read_b64_tr_b16 v[170:171], v228 offset:6976\n"
    "v_mfma_f32_16x16x32_bf16 v[72:75], v[124:127], v[104:107], v[72:75]\n"
    "ds_read_b64_tr_b16 v[172:173], v228 offset:18496\n"
    "v_mfma_f32_16x16x32_bf16 v[76:79], v[124:127], v[108:111], v[76:79]\n"
    "ds_read_b64_tr_b16 v[174:175], v228 offset:19264\n"
    "s_waitcnt lgkmcnt(15)\n"
    "v_mfma_f32_16x16x32_bf16 v[32:35], v[128:131], v[96:99], v[32:35]\n"
    "v_mfma_f32_16x16x32_bf16 v[36:39], v[128:131], v[100:103], v[36:39]\n"
    "v_mfma_f32_16x16x32_bf16 v[80:83], v[128:131], v[104:107], v[80:83]\n"
    "v_mfma_f32_16x16x32_bf16 v[84:87], v[128:131], v[108:111], v[84:87]\n"
    "v_mfma_f32_16x16x32_bf16 v[40:43], v[132:135], v[96:99], v[40:43]\n"
    "v_mfma_f32_16x16x32_bf16 v[44:47], v[132:135], v[100:103], v[44:47]\n"
    "v_mfma_f32_16x16x32_bf16 v[88:91], v[132:135], v[104:107], v[88:91]\n"
    "v_mfma_f32_16x16x32_bf16 v[92:95], v[132:135], v[108:111], v[92:95]\n"
    "s_waitcnt lgkmcnt(13)\n"
    "v_mfma_f32_16x16x32_bf16 v[0:3], v[152:155], v[136:139], v[0:3]\n"
    "s_waitcnt lgkmcnt(12)\n"
    "v_mfma_f32_16x16x32_bf16 v[4:7], v[152:155], v[140:143], v[4:7]\n"
    "s_waitcnt lgkmcnt(11)\n"
    "v_mfma_f32_16x16x32_bf16 v[48:51], v[152:155], v[144:147], v[48:51]\n"
    "s_waitcnt lgkmcnt(10)\n"
    "v_mfma_f32_16x16x32_bf16 v[52:55], v[152:155], v[148:151], v[52:55]\n"
    "s_waitcnt lgkmcnt(8)\n"
    "v_mfma_f32_16x16x32_bf16 v[8:11], v[156:159], v[136:139], v[8:11]\n"
    "v_mfma_f32_16x16x32_bf16 v[12:15], v[156:159], v[140:143], v[12:15]\n"
    "v_mfma_f32_16x16x32_bf16 v[56:59], v[156:159], v[144:147], v[56:59]\n"
    "v_mfma_f32_16x16x32_bf16 v[60:63], v[156:159], v[148:151], v[60:63]\n"
    "s_waitcnt lgkmcnt(0)\n"
    "s_barrier\n"
    "ds_read_b128 v[96:99], v224 offset:40960\n"
    "ds_read_b64_tr_b16 v[112:113], v228 offset:40960\n"
    "v_mfma_f32_16x16x32_bf16 v[16:19], v[160:163], v[136:139], v[16:19]\n"
    "ds_read_b64_tr_b16 v[114:115], v228 offset:41728\n"
    "v_mfma_f32_16x16x32_bf16 v[20:23], v[160:163], v[140:143], v[20:23]\n"
    "ds_read_b128 v[100:103], v224 offset:43008\n"
    "v_mfma_f32_16x16x32_bf16 v[64:67], v[160:163], v[144:147], v[64:67]\n"
    "ds_read_b128 v[104:107], v224 offset:45056\n"
    "v_mfma_f32_16x16x32_bf16 v[68:71], v[160:163], v[148:151], v[68:71]\n"
    "ds_read_b128 v[108:111], v224 offset:47104\n"
    "v_mfma_f32_16x16x32_bf16 v[24:27], v[164:167], v[136:139], v[24:27]\n"
    "ds_read_b64_tr_b16 v[116:117], v228 offset:53248\n"
    "v_mfma_f32_16x16x32_bf16 v[28:31], v[164:167], v[140:143], v[28:31]\n"
    "ds_read_b64_tr_b16 v[118:119], v228 offset:54016\n"
    "v_mfma_f32_16x16x32_bf16 v[72:75], v[164:167], v[144:147], v[72:75]\n"
    "ds_read_b64_tr_b16 v[120:121], v229 offset:40992\n"
    "v_mfma_f32_16x16x32_bf16 v[76:79], v[164:167], v[148:151], v[76:79]\n"
    "ds_read_b64_tr_b16 v[122:123], v229 offset:41760\n"
    "v_mfma_f32_16x16x32_bf16 v[32:35], v[168:171], v[136:139], v[32:35]\n"
    "ds_read_b64_tr_b16 v[124:125], v229 offset:53280\n"
    "v_mfma_f32_16x16x32_bf16 v[36:39], v[168:171], v[140:143], v[36:39]\n"
    "ds_read_b64_tr_b16 v[126:127], v229 offset:54048\n"
    "v_mfma_f32_16x16x32_bf16 v[80:83], v[168:171], v[144:147], v[80:83]\n"
    "ds_read_b64_tr_b16 v[128:129], v228 offset:41024\n"
    "v_mfma_f32_16x16x32_bf16 v[84:87], v[168:171], v[148:151], v[84:87]\n"
    "ds_read_b64_tr_b16 v[130:131], v228 offset:41792\n"
    "v_mfma_f32_16x16x32_bf16 v[40:43], v[172:175], v[136:139], v[40:43]\n"
    "ds_read_b64_tr_b16 v[132:133], v228 offset:53312\n"
    "v_mfma_f32_16x16x32_bf16 v[44:47], v[172:175], v[140:143], v[44:47]\n"
    "ds_read_b64_tr_b16 v[134:135], v228 offset:54080\n"
    "v_mfma_f32_16x16x32_bf16 v[88:91], v[172:175], v[144:147], v[88:91]\n"
    "v_mfma_f32_16x16x32_bf16 v[92:95], v[172:175], v[148:151], v[92:95]\n"
    "s_waitcnt lgkmcnt(13)\n"
    "v_mfma_f32_16x16x32_bf16 v[0:3], v[112:115], v[96:99], v[0:3]\n"
    "ds_read_b128 v[136:139], v225 offset:40960\n"
    "s_waitcnt lgkmcnt(13)\n"
    "v_mfma_f32_16x16x32_bf16 v[4:7], v[112:115], v[100:103], v[4:7]\n"
    "ds_read_b64_tr_b16 v[152:153], v228 offset:47104\n"
    "s_waitcnt lgkmcnt(13)\n"
    "v_mfma_f32_16x16x32_bf16 v[48:51], v[112:115], v[104:107], v[48:51]\n"
    "ds_read_b64_tr_b16 v[154:155], v228 offset:47872\n"
    "s_waitcnt lgkmcnt(13)\n"
    "v_mfma_f32_16x16x32_bf16 v[52:55], v[112:115], v[108:111], v[52:55]\n"
    "ds_read_b128 v[140:143], v225 offset:43008\n"
    "s_waitcnt lgkmcnt(12)\n"
    "v_mfma_f32_16x16x32_bf16 v[8:11], v[116:119], v[96:99], v[8:11]\n"
    "ds_read_b128 v[144:147], v225 offset:45056\n"
    "v_mfma_f32_16x16x32_bf16 v[12:15], v[116:119], v[100:103], v[12:15]\n"
    "ds_read_b128 v[148:151], v225 offset:47104\n"
    "v_mfma_f32_16x16x32_bf16 v[56:59], v[116:119], v[104:107], v[56:59]\n"
    "ds_read_b64_tr_b16 v[156:157], v228 offset:59392\n"
    "v_mfma_f32_16x16x32_bf16 v[60:63], v[116:119], v[108:111], v[60:63]\n"
    "ds_read_b64_tr_b16 v[158:159], v228 offset:60160\n"
    "s_waitcnt lgkmcnt(14)\n"
    "v_mfma_f32_16x16x32_bf16 v[16:19], v[120:123], v[96:99], v[16:19]\n"
    "ds_read_b64_tr_b16 v[160:161], v229 offset:47136\n"
    "v_mfma_f32_16x16x32_bf16 v[20:23], v[120:123], v[100:103], v[20:23]\n"
    "ds_read_b64_tr_b16 v[162:163], v229 offset:47904\n"
    "v_mfma_f32_16x16x32_bf16 v[64:67], v[120:123], v[104:107], v[64:67]\n"
    "ds_read_b64_tr_b16 v[164:165], v229 offset:59424\n"
    "v_mfma_f32_16x16x32_bf16 v[68:71], v[120:123], v[108:111], v[68:71]\n"
    "ds_read_b64_tr_b16 v[166:167], v229 offset:60192\n"
    "s_waitcnt lgkmcnt(15)\n"
    "v_mfma_f32_16x16x32_bf16 v[24:27], v[124:127], v[96:99], v[24:27]\n"
    "ds_read_b64_tr_b16 v[168:169], v228 offset:47168\n"
    "v_mfma_f32_16x16x32_bf16 v[28:31], v[124:127], v[100:103], v[28:31]\n"
    "ds_read_b64_tr_b16 v[170:171], v228 offset:47936\n"
    "v_mfma_f32_16x16x32_bf16 v[72:75], v[124:127], v[104:107], v[72:75]\n"
    "ds_read_b64_tr_b16 v[172:173], v228 offset:59456\n"
    "v_mfma_f32_16x16x32_bf16 v[76:79], v[124:127], v[108:111], v[76:79]\n"
    "ds_read_b64_tr_b16 v[174:175], v228 offset:60224\n"
    "s_waitcnt lgkmcnt(15)\n"
    "v_mfma_f32_16x16x32_bf16 v[32:35], v[128:131], v[96:99], v[32:35]\n"
    "v_mfma_f32_16x16x32_bf16 v[36:39], v[128:131], v[100:103], v[36:39]\n"
    "v_mfma_f32_16x16x32_bf16 v[80:83], v[128:131], v[104:107], v[80:83]\n"
    "v_mfma_f32_16x16x32_bf16 v[84:87], v[128:131], v[108:111], v[84:87]\n"
    "v_mfma_f32_16x16x32_bf16 v[40:43], v[132:135], v[96:99], v[40:43]\n"
    "v_mfma_f32_16x16x32_bf16 v[44:47], v[132:135], v[100:103], v[44:47]\n"
    "v_mfma_f32_16x16x32_bf16 v[88:91], v[132:135], v[104:107], v[88:91]\n"
    "v_mfma_f32_16x16x32_bf16 v[92:95], v[132:135], v[108:111], v[92:95]\n"
    "s_waitcnt lgkmcnt(13)\n"
    "v_mfma_f32_16x16x32_bf16 v[0:3], v[152:155], v[136:139], v[0:3]\n"
    "s_waitcnt lgkmcnt(12)\n"
    "v_mfma_f32_16x16x32_bf16 v[4:7], v[152:155], v[140:143], v[4:7]\n"
    "s_waitcnt lgkmcnt(11)\n"
    "v_mfma_f32_16x16x32_bf16 v[48:51], v[152:155], v[144:147], v[48:51]\n"
    "s_waitcnt lgkmcnt(10)\n"
    "v_mfma_f32_16x16x32_bf16 v[52:55], v[152:155], v[148:151], v[52:55]\n"
    "s_waitcnt lgkmcnt(8)\n"
    "v_mfma_f32_16x16x32_bf16 v[8:11], v[156:159], v[136:139], v[8:11]\n"
    "v_mfma_f32_16x16x32_bf16 v[12:15], v[156:159], v[140:143], v[12:15]\n"
    "v_mfma_f32_16x16x32_bf16 v[56:59], v[156:159], v[144:147], v[56:59]\n"
    "v_mfma_f32_16x16x32_bf16 v[60:63], v[156:159], v[148:151], v[60:63]\n"
    "s_waitcnt lgkmcnt(0)\n"
    "s_barrier\n"
    "ds_read_b128 v[96:99], v226 offset:0\n"
    "ds_read_b64_tr_b16 v[112:113], v230 offset:0\n"
    "v_mfma_f32_16x16x32_bf16 v[16:19], v[160:163], v[136:139], v[16:19]\n"
    "ds_read_b64_tr_b16 v[114:115], v230 offset:768\n"
    "v_mfma_f32_16x16x32_bf16 v[20:23], v[160:163], v[140:143], v[20:23]\n"
    "ds_read_b128 v[100:103], v226 offset:2048\n"
    "v_mfma_f32_16x16x32_bf16 v[64:67], v[160:163], v[144:147], v[64:67]\n"
    "ds_read_b128 v[104:107], v226 offset:4096\n"
    "v_mfma_f32_16x16x32_bf16 v[68:71], v[160:163], v[148:151], v[68:71]\n"
    "ds_read_b128 v[108:111], v226 offset:6144\n"
    "v_mfma_f32_16x16x32_bf16 v[24:27], v[164:167], v[136:139], v[24:27]\n"
    "ds_read_b64_tr_b16 v[116:117], v230 offset:12288\n"
    "v_mfma_f32_16x16x32_bf16 v[28:31], v[164:167], v[140:143], v[28:31]\n"
    "ds_read_b64_tr_b16 v[118:119], v230 offset:13056\n"
    "v_mfma_f32_16x16x32_bf16 v[72:75], v[164:167], v[144:147], v[72:75]\n"
    "ds_read_b64_tr_b16 v[120:121], v231 offset:32\n"
    "v_mfma_f32_16x16x32_bf16 v[76:79], v[164:167], v[148:151], v[76:79]\n"
    "ds_read_b64_tr_b16 v[122:123], v231 offset:800\n"
    "v_mfma_f32_16x16x32_bf16 v[32:35], v[168:171], v[136:139], v[32:35]\n"
    "ds_read_b64_tr_b16 v[124:125], v231 offset:12320\n"
    "v_mfma_f32_16x16x32_bf16 v[36:39], v[168:171], v[140:143], v[36:39]\n"
    "ds_read_b64_tr_b16 v[126:127], v231 offset:13088\n"
    "v_mfma_f32_16x16x32_bf16 v[80:83], v[168:171], v[144:147], v[80:83]\n"
    "ds_read_b64_tr_b16 v[128:129], v230 offset:64\n"
    "v_mfma_f32_16x16x32_bf16 v[84:87], v[168:171], v[148:151], v[84:87]\n"
    "ds_read_b64_tr_b16 v[130:131], v230 offset:832\n"
    "v_mfma_f32_16x16x32_bf16 v[40:43], v[172:175], v[136:139], v[40:43]\n"
    "ds_read_b64_tr_b16 v[132:133], v230 offset:12352\n"
    "v_mfma_f32_16x16x32_bf16 v[44:47], v[172:175], v[140:143], v[44:47]\n"
    "ds_read_b64_tr_b16 v[134:135], v230 offset:13120\n"
    "v_mfma_f32_16x16x32_bf16 v[88:91], v[172:175], v[144:147], v[88:91]\n"
    "v_mfma_f32_16x16x32_bf16 v[92:95], v[172:175], v[148:151], v[92:95]\n"
    "s_waitcnt lgkmcnt(13)\n"
    "v_mfma_f32_16x16x32_bf16 v[0:3], v[112:115], v[96:99], v[0:3]\n"
    "ds_read_b128 v[136:139], v227 offset:0\n"
    "s_waitcnt lgkmcnt(13)\n"
    "v_mfma_f32_16x16x32_bf16 v[4:7], v[112:115], v[100:103], v[4:7]\n"
    "ds_read_b64_tr_b16 v[152:153], v230 offset:6144\n"
    "s_waitcnt lgkmcnt(13)\n"
    "v_mfma_f32_16x16x32_bf16 v[48:51], v[112:115], v[104:107], v[48:51]\n"
    "ds_read_b64_tr_b16 v[154:155], v230 offset:6912\n"
    "s_waitcnt lgkmcnt(13)\n"
    "v_mfma_f32_16x16x32_bf16 v[52:55], v[112:115], v[108:111], v[52:55]\n"
    "ds_read_b128 v[140:143], v227 offset:2048\n"
    "s_waitcnt lgkmcnt(12)\n"
    "v_mfma_f32_16x16x32_bf16 v[8:11], v[116:119], v[96:99], v[8:11]\n"
    "ds_read_b128 v[144:147], v227 offset:4096\n"
    "v_mfma_f32_16x16x32_bf16 v[12:15], v[116:119], v[100:103], v[12:15]\n"
    "ds_read_b128 v[148:151], v227 offset:6144\n"
    "v_mfma_f32_16x16x32_bf16 v[56:59], v[116:119], v[104:107], v[56:59]\n"
    "ds_read_b64_tr_b16 v[156:157], v230 offset:18432\n"
    "v_mfma_f32_16x16x32_bf16 v[60:63], v[116:119], v[108:111], v[60:63]\n"
    "ds_read_b64_tr_b16 v[158:159], v230 offset:19200\n"
    "s_waitcnt lgkmcnt(14)\n"
    "v_mfma_f32_16x16x32_bf16 v[16:19], v[120:123], v[96:99], v[16:19]\n"
    "ds_read_b64_tr_b16 v[160:161], v231 offset:6176\n"
    "v_mfma_f32_16x16x32_bf16 v[20:23], v[120:123], v[100:103], v[20:23]\n"
    "ds_read_b64_tr_b16 v[162:163], v231 offset:6944\n"
    "v_mfma_f32_16x16x32_bf16 v[64:67], v[120:123], v[104:107], v[64:67]\n"
    "ds_read_b64_tr_b16 v[164:165], v231 offset:18464\n"
    "v_mfma_f32_16x16x32_bf16 v[68:71], v[120:123], v[108:111], v[68:71]\n"
    "ds_read_b64_tr_b16 v[166:167], v231 offset:19232\n"
    "s_waitcnt lgkmcnt(15)\n"
    "v_mfma_f32_16x16x32_bf16 v[24:27], v[124:127], v[96:99], v[24:27]\n"
    "ds_read_b64_tr_b16 v[168:169], v230 offset:6208\n"
    "v_mfma_f32_16x16x32_bf16 v[28:31], v[124:127], v[100:103], v[28:31]\n"
    "ds_read_b64_tr_b16 v[170:171], v230 offset:6976\n"
    "v_mfma_f32_16x16x32_bf16 v[72:75], v[124:127], v[104:107], v[72:75]\n"
    "ds_read_b64_tr_b16 v[172:173], v230 offset:18496\n"
    "v_mfma_f32_16x16x32_bf16 v[76:79], v[124:127], v[108:111], v[76:79]\n"
    "ds_read_b64_tr_b16 v[174:175], v230 offset:19264\n"
    "s_waitcnt lgkmcnt(15)\n"
    "v_mfma_f32_16x16x32_bf16 v[32:35], v[128:131], v[96:99], v[32:35]\n"
    "v_mfma_f32_16x16x32_bf16 v[36:39], v[128:131], v[100:103], v[36:39]\n"
    "v_mfma_f32_16x16x32_bf16 v[80:83], v[128:131], v[104:107], v[80:83]\n"
    "v_mfma_f32_16x16x32_bf16 v[84:87], v[128:131], v[108:111], v[84:87]\n"
    "v_mfma_f32_16x16x32_bf16 v[40:43], v[132:135], v[96:99], v[40:43]\n"
    "v_mfma_f32_16x16x32_bf16 v[44:47], v[132:135], v[100:103], v[44:47]\n"
    "v_mfma_f32_16x16x32_bf16 v[88:91], v[132:135], v[104:107], v[88:91]\n"
    "v_mfma_f32_16x16x32_bf16 v[92:95], v[132:135], v[108:111], v[92:95]\n"
    "s_waitcnt lgkmcnt(13)\n"
    "v_mfma_f32_16x16x32_bf16 v[0:3], v[152:155], v[136:139], v[0:3]\n"
    "s_waitcnt lgkmcnt(12)\n"
    "v_mfma_f32_16x16x32_bf16 v[4:7], v[152:155], v[140:143], v[4:7]\n"
    "s_waitcnt lgkmcnt(11)\n"
    "v_mfma_f32_16x16x32_bf16 v[48:51], v[152:155], v[144:147], v[48:51]\n"
    "s_waitcnt lgkmcnt(10)\n"
    "v_mfma_f32_16x16x32_bf16 v[52:55], v[152:155], v[148:151], v[52:55]\n"
    "s_waitcnt lgkmcnt(8)\n"
    "v_mfma_f32_16x16x32_bf16 v[8:11], v[156:159], v[136:139], v[8:11]\n"
    "v_mfma_f32_16x16x32_bf16 v[12:15], v[156:159], v[140:143], v[12:15]\n"
    "v_mfma_f32_16x16x32_bf16 v[56:59], v[156:159], v[144:147], v[56:59]\n"
    "v_mfma_f32_16x16x32_bf16 v[60:63], v[156:159], v[148:151], v[60:63]\n"
    "s_waitcnt lgkmcnt(0)\n"
    "s_barrier\n"
    "ds_read_b128 v[96:99], v224 offset:0\n"
    "ds_read_b64_tr_b16 v[112:113], v228 offset:0\n"
    "v_mfma_f32_16x16x32_bf16 v[16:19], v[160:163], v[136:139], v[16:19]\n"
    "ds_read_b64_tr_b16 v[114:115], v228 offset:768\n"
    "v_mfma_f32_16x16x32_bf16 v[20:23], v[160:163], v[140:143], v[20:23]\n"
    "ds_read_b128 v[100:103], v224 offset:2048\n"
    "v_mfma_f32_16x16x32_bf16 v[64:67], v[160:163], v[144:147], v[64:67]\n"
    "ds_read_b128 v[104:107], v224 offset:4096\n"
    "v_mfma_f32_16x16x32_bf16 v[68:71], v[160:163], v[148:151], v[68:71]\n"
    "ds_read_b128 v[108:111], v224 offset:6144\n"
    "v_mfma_f32_16x16x32_bf16 v[24:27], v[164:167], v[136:139], v[24:27]\n"
    "ds_read_b64_tr_b16 v[116:117], v228 offset:12288\n"
    "v_mfma_f32_16x16x32_bf16 v[28:31], v[164:167], v[140:143], v[28:31]\n"
    "ds_read_b64_tr_b16 v[118:119], v228 offset:13056\n"
    "v_mfma_f32_16x16x32_bf16 v[72:75], v[164:167], v[144:147], v[72:75]\n"
    "ds_read_b64_tr_b16 v[120:121], v229 offset:32\n"
    "v_mfma_f32_16x16x32_bf16 v[76:79], v[164:167], v[148:151], v[76:79]\n"
    "ds_read_b64_tr_b16 v[122:123], v229 offset:800\n"
    "v_mfma_f32_16x16x32_bf16 v[32:35], v[168:171], v[136:139], v[32:35]\n"
    "ds_read_b64_tr_b16 v[124:125], v229 offset:12320\n"
    "v_mfma_f32_16x16x32_bf16 v[36:39], v[168:171], v[140:143], v[36:39]\n"
    "ds_read_b64_tr_b16 v[126:127], v229 offset:13088\n"
    "v_mfma_f32_16x16x32_bf16 v[80:83], v[168:171], v[144:147], v[80:83]\n"
    "ds_read_b64_tr_b16 v[128:129], v228 offset:64\n"
    "v_mfma_f32_16x16x32_bf16 v[84:87], v[168:171], v[148:151], v[84:87]\n"
    "ds_read_b64_tr_b16 v[130:131], v228 offset:832\n"
    "v_mfma_f32_16x16x32_bf16 v[40:43], v[172:175], v[136:139], v[40:43]\n"
    "ds_read_b64_tr_b16 v[132:133], v228 offset:12352\n"
    "v_mfma_f32_16x16x32_bf16 v[44:47], v[172:175], v[140:143], v[44:47]\n"
    "ds_read_b64_tr_b16 v[134:135], v228 offset:13120\n"
    "v_mfma_f32_16x16x32_bf16 v[88:91], v[172:175], v[144:147], v[88:91]\n"
    "v_mfma_f32_16x16x32_bf16 v[92:95], v[172:175], v[148:151], v[92:95]\n"
    "s_waitcnt lgkmcnt(13)\n"
    "v_mfma_f32_16x16x32_bf16 v[0:3], v[112:115], v[96:99], v[0:3]\n"
    "ds_read_b128 v[136:139], v225 offset:0\n"
    "s_waitcnt lgkmcnt(13)\n"
    "v_mfma_f32_16x16x32_bf16 v[4:7], v[112:115], v[100:103], v[4:7]\n"
    "ds_read_b64_tr_b16 v[152:153], v228 offset:6144\n"
    "s_waitcnt lgkmcnt(13)\n"
    "v_mfma_f32_16x16x32_bf16 v[48:51], v[112:115], v[104:107], v[48:51]\n"
    "ds_read_b64_tr_b16 v[154:155], v228 offset:6912\n"
    "s_waitcnt lgkmcnt(13)\n"
    "v_mfma_f32_16x16x32_bf16 v[52:55], v[112:115], v[108:111], v[52:55]\n"
    "ds_read_b128 v[140:143], v225 offset:2048\n"
    "s_waitcnt lgkmcnt(12)\n"
    "v_mfma_f32_16x16x32_bf16 v[8:11], v[116:119], v[96:99], v[8:11]\n"
    "ds_read_b128 v[144:147], v225 offset:4096\n"
    "v_mfma_f32_16x16x32_bf16 v[12:15], v[116:119], v[100:103], v[12:15]\n"
    "ds_read_b128 v[148:151], v225 offset:6144\n"
    "v_mfma_f32_16x16x32_bf16 v[56:59], v[116:119], v[104:107], v[56:59]\n"
    "ds_read_b64_tr_b16 v[156:157], v228 offset:18432\n"
    "v_mfma_f32_16x16x32_bf16 v[60:63], v[116:119], v[108:111], v[60:63]\n"
    "ds_read_b64_tr_b16 v[158:159], v228 offset:19200\n"
    "s_waitcnt lgkmcnt(14)\n"
    "v_mfma_f32_16x16x32_bf16 v[16:19], v[120:123], v[96:99], v[16:19]\n"
    "ds_read_b64_tr_b16 v[160:161], v229 offset:6176\n"
    "v_mfma_f32_16x16x32_bf16 v[20:23], v[120:123], v[100:103], v[20:23]\n"
    "ds_read_b64_tr_b16 v[162:163], v229 offset:6944\n"
    "v_mfma_f32_16x16x32_bf16 v[64:67], v[120:123], v[104:107], v[64:67]\n"
    "ds_read_b64_tr_b16 v[164:165], v229 offset:18464\n"
    "v_mfma_f32_16x16x32_bf16 v[68:71], v[120:123], v[108:111], v[68:71]\n"
    "ds_read_b64_tr_b16 v[166:167], v229 offset:19232\n"
    "s_waitcnt lgkmcnt(15)\n"
    "v_mfma_f32_16x16x32_bf16 v[24:27], v[124:127], v[96:99], v[24:27]\n"
    "ds_read_b64_tr_b16 v[168:169], v228 offset:6208\n"
    "v_mfma_f32_16x16x32_bf16 v[28:31], v[124:127], v[100:103], v[28:31]\n"
    "ds_read_b64_tr_b16 v[170:171], v228 offset:6976\n"
    "v_mfma_f32_16x16x32_bf16 v[72:75], v[124:127], v[104:107], v[72:75]\n"
    "ds_read_b64_tr_b16 v[172:173], v228 offset:18496\n"
    "v_mfma_f32_16x16x32_bf16 v[76:79], v[124:127], v[108:111], v[76:79]\n"
    "ds_read_b64_tr_b16 v[174:175], v228 offset:19264\n"
    "s_waitcnt lgkmcnt(15)\n"
    "v_mfma_f32_16x16x32_bf16 v[32:35], v[128:131], v[96:99], v[32:35]\n"
    "v_mfma_f32_16x16x32_bf16 v[36:39], v[128:131], v[100:103], v[36:39]\n"
    "v_mfma_f32_16x16x32_bf16 v[80:83], v[128:131], v[104:107], v[80:83]\n"
    "v_mfma_f32_16x16x32_bf16 v[84:87], v[128:131], v[108:111], v[84:87]\n"
    "v_mfma_f32_16x16x32_bf16 v[40:43], v[132:135], v[96:99], v[40:43]\n"
    "v_mfma_f32_16x16x32_bf16 v[44:47], v[132:135], v[100:103], v[44:47]\n"
    "v_mfma_f32_16x16x32_bf16 v[88:91], v[132:135], v[104:107], v[88:91]\n"
    "v_mfma_f32_16x16x32_bf16 v[92:95], v[132:135], v[108:111], v[92:95]\n"
    "s_waitcnt lgkmcnt(13)\n"
    "v_mfma_f32_16x16x32_bf16 v[0:3], v[152:155], v[136:139], v[0:3]\n"
    "s_waitcnt lgkmcnt(12)\n"
    "v_mfma_f32_16x16x32_bf16 v[4:7], v[152:155], v[140:143], v[4:7]\n"
    "s_waitcnt lgkmcnt(11)\n"
    "v_mfma_f32_16x16x32_bf16 v[48:51], v[152:155], v[144:147], v[48:51]\n"
    "s_waitcnt lgkmcnt(10)\n"
    "v_mfma_f32_16x16x32_bf16 v[52:55], v[152:155], v[148:151], v[52:55]\n"
    "s_waitcnt lgkmcnt(8)\n"
    "v_mfma_f32_16x16x32_bf16 v[8:11], v[156:159], v[136:139], v[8:11]\n"
    "v_mfma_f32_16x16x32_bf16 v[12:15], v[156:159], v[140:143], v[12:15]\n"
    "v_mfma_f32_16x16x32_bf16 v[56:59], v[156:159], v[144:147], v[56:59]\n"
    "v_mfma_f32_16x16x32_bf16 v[60:63], v[156:159], v[148:151], v[60:63]\n"
    "s_waitcnt lgkmcnt(6)\n"
    "v_mfma_f32_16x16x32_bf16 v[16:19], v[160:163], v[136:139], v[16:19]\n"
    "v_mfma_f32_16x16x32_bf16 v[20:23], v[160:163], v[140:143], v[20:23]\n"
    "v_mfma_f32_16x16x32_bf16 v[64:67], v[160:163], v[144:147], v[64:67]\n"
    "v_mfma_f32_16x16x32_bf16 v[68:71], v[160:163], v[148:151], v[68:71]\n"
    "s_waitcnt lgkmcnt(4)\n"
    "v_mfma_f32_16x16x32_bf16 v[24:27], v[164:167], v[136:139], v[24:27]\n"
    "v_mfma_f32_16x16x32_bf16 v[28:31], v[164:167], v[140:143], v[28:31]\n"
    "v_mfma_f32_16x16x32_bf16 v[72:75], v[164:167], v[144:147], v[72:75]\n"
    "v_mfma_f32_16x16x32_bf16 v[76:79], v[164:167], v[148:151], v[76:79]\n"
    "s_waitcnt lgkmcnt(2)\n"
    "v_mfma_f32_16x16x32_bf16 v[32:35], v[168:171], v[136:139], v[32:35]\n"
    "v_mfma_f32_16x16x32_bf16 v[36:39], v[168:171], v[140:143], v[36:39]\n"
    "v_mfma_f32_16x16x32_bf16 v[80:83], v[168:171], v[144:147], v[80:83]\n"
    "v_mfma_f32_16x16x32_bf16 v[84:87], v[168:171], v[148:151], v[84:87]\n"
    "s_waitcnt lgkmcnt(0)\n"
    "s_setprio 2\n"
    "s_barrier\n"
    "ds_read_b128 v[96:99], v224 offset:40960\n"
    "ds_read_b64_tr_b16 v[112:113], v228 offset:40960\n"
    "ds_read_b64_tr_b16 v[114:115], v228 offset:41728\n"
    "v_mfma_f32_16x16x32_bf16 v[40:43], v[172:175], v[136:139], v[40:43]\n"
    "ds_read_b128 v[100:103], v224 offset:43008\n"
    "v_mfma_f32_16x16x32_bf16 v[44:47], v[172:175], v[140:143], v[44:47]\n"
    "ds_read_b64_tr_b16 v[116:117], v228 offset:53248\n"
    "v_mfma_f32_16x16x32_bf16 v[88:91], v[172:175], v[144:147], v[88:91]\n"
    "ds_read_b64_tr_b16 v[118:119], v228 offset:54016\n"
    "v_mfma_f32_16x16x32_bf16 v[92:95], v[172:175], v[148:151], v[92:95]\n"
    "ds_read_b128 v[136:139], v225 offset:40960\n"
    "ds_read_b64_tr_b16 v[152:153], v228 offset:47104\n"
    "ds_read_b64_tr_b16 v[154:155], v228 offset:47872\n"
    "ds_read_b128 v[140:143], v225 offset:43008\n"
    "ds_read_b64_tr_b16 v[156:157], v228 offset:59392\n"
    "ds_read_b64_tr_b16 v[158:159], v228 offset:60160\n"
    "s_waitcnt lgkmcnt(9)\n"
    "v_mfma_f32_16x16x32_bf16 v[0:3], v[112:115], v[96:99], v[0:3]\n"
    "ds_read_b64_tr_b16 v[120:121], v229 offset:40992\n"
    "ds_read_b64_tr_b16 v[122:123], v229 offset:41760\n"
    "s_waitcnt lgkmcnt(10)\n"
    "v_mfma_f32_16x16x32_bf16 v[4:7], v[112:115], v[100:103], v[4:7]\n"
    "ds_read_b64_tr_b16 v[124:125], v229 offset:53280\n"
    "ds_read_b64_tr_b16 v[126:127], v229 offset:54048\n"
    "s_waitcnt lgkmcnt(10)\n"
    "v_mfma_f32_16x16x32_bf16 v[8:11], v[116:119], v[96:99], v[8:11]\n"
    "ds_read_b64_tr_b16 v[160:161], v229 offset:47136\n"
    "ds_read_b64_tr_b16 v[162:163], v229 offset:47904\n"
    "v_mfma_f32_16x16x32_bf16 v[12:15], v[116:119], v[100:103], v[12:15]\n"
    "ds_read_b64_tr_b16 v[164:165], v229 offset:59424\n"
    "ds_read_b64_tr_b16 v[166:167], v229 offset:60192\n"
    "s_waitcnt lgkmcnt(11)\n"
    "v_mfma_f32_16x16x32_bf16 v[0:3], v[152:155], v[136:139], v[0:3]\n"
    "ds_read_b64_tr_b16 v[128:129], v228 offset:41024\n"
    "ds_read_b64_tr_b16 v[130:131], v228 offset:41792\n"
    "s_waitcnt lgkmcnt(12)\n"
    "v_mfma_f32_16x16x32_bf16 v[4:7], v[152:155], v[140:143], v[4:7]\n"
    "ds_read_b64_tr_b16 v[132:133], v228 offset:53312\n"
    "ds_read_b64_tr_b16 v[134:135], v228 offset:54080\n"
    "s_waitcnt lgkmcnt(12)\n"
    "v_mfma_f32_16x16x32_bf16 v[8:11], v[156:159], v[136:139], v[8:11]\n"
    "ds_read_b64_tr_b16 v[168:169], v228 offset:47168\n"
    "ds_read_b64_tr_b16 v[170:171], v228 offset:47936\n"
    "v_mfma_f32_16x16x32_bf16 v[12:15], v[156:159], v[140:143], v[12:15]\n"
    "ds_read_b64_tr_b16 v[172:173], v228 offset:59456\n"
    "ds_read_b64_tr_b16 v[174:175], v228 offset:60224\n"
    "s_waitcnt lgkmcnt(14)\n"
    "v_mfma_f32_16x16x32_bf16 v[16:19], v[120:123], v[96:99], v[16:19]\n"
    "ds_read_b128 v[104:107], v224 offset:45056\n"
    "ds_read_b128 v[108:111], v224 offset:47104\n"
    "v_mfma_f32_16x16x32_bf16 v[20:23], v[120:123], v[100:103], v[20:23]\n"
    "ds_read_b128 v[144:147], v225 offset:45056\n"
    "ds_read_b128 v[148:151], v225 offset:47104\n"
    "s_waitcnt lgkmcnt(15)\n"
    "v_mfma_f32_16x16x32_bf16 v[24:27], v[124:127], v[96:99], v[24:27]\n"
    "v_mul_f32 v184, v0, v0\n"
    "v_mul_f32_e64 v185, v1, v1\n"
    "v_mfma_f32_16x16x32_bf16 v[28:31], v[124:127], v[100:103], v[28:31]\n"
    "v_mul_f32 v186, v2, v2\n"
    "v_mul_f32_e64 v187, v3, v3\n"
    "s_waitcnt lgkmcnt(14)\n"
    "v_mfma_f32_16x16x32_bf16 v[16:19], v[160:163], v[136:139], v[16:19]\n"
    "v_mul_f32 v188, v4, v4\n"
    "v_mul_f32 v189, v5, v5\n"
    "v_mfma_f32_16x16x32_bf16 v[20:23], v[160:163], v[140:143], v[20:23]\n"
    "v_mul_f32 v190, v6, v6\n"
    "v_mul_f32_e64 v191, v7, v7\n"
    "s_waitcnt lgkmcnt(12)\n"
    "v_mfma_f32_16x16x32_bf16 v[24:27], v[164:167], v[136:139], v[24:27]\n"
    "s_waitcnt lgkmcnt(0)\n"
    "s_barrier\n"
    "v_fma_f32 v184, v184, v246, v247\n"
    "v_fma_f32 v185, v185, v246, v247\n"
    "v_mfma_f32_16x16x32_bf16 v[28:31], v[164:167], v[140:143], v[28:31]\n"
    "v_fma_f32 v186, v186, v246, v247\n"
    "v_fma_f32 v187, v187, v246, v247\n"
    "v_mfma_f32_16x16x32_bf16 v[32:35], v[128:131], v[96:99], v[32:35]\n"
    "v_fma_f32 v188, v188, v246, v247\n"
    "v_fma_f32 v189, v189, v246, v247\n"
    "v_mfma_f32_16x16x32_bf16 v[36:39], v[128:131], v[100:103], v[36:39]\n"
    "v_fma_f32 v190, v190, v246, v247\n"
    "v_fma_f32 v191, v191, v246, v247\n"
    "v_mfma_f32_16x16x32_bf16 v[40:43], v[132:135], v[96:99], v[40:43]\n"
    "v_mul_f32 v184, v184, v0\n"
    "v_mul_f32 v185, v185, v1\n"
    "v_mfma_f32_16x16x32_bf16 v[44:47], v[132:135], v[100:103], v[44:47]\n"
    "v_mul_f32 v186, v186, v2\n"
    "v_mul_f32 v187, v187, v3\n"
    "v_mfma_f32_16x16x32_bf16 v[32:35], v[168:171], v[136:139], v[32:35]\n"
    "v_mul_f32 v188, v188, v4\n"
    "v_mul_f32 v189, v189, v5\n"
    "v_mfma_f32_16x16x32_bf16 v[36:39], v[168:171], v[140:143], v[36:39]\n"
    "v_mul_f32 v190, v190, v6\n"
    "v_mul_f32 v191, v191, v7\n"
    "v_mfma_f32_16x16x32_bf16 v[40:43], v[172:175], v[136:139], v[40:43]\n"
    "v_exp_f32_e64 v184, v184\n"
    "v_mfma_f32_16x16x32_bf16 v[44:47], v[172:175], v[140:143], v[44:47]\n"
    "v_exp_f32_e64 v185, v185\n"
    "v_mfma_f32_16x16x32_bf16 v[48:51], v[112:115], v[104:107], v[48:51]\n"
    "v_exp_f32_e64 v186, v186\n"
    "v_mfma_f32_16x16x32_bf16 v[52:55], v[112:115], v[108:111], v[52:55]\n"
    "v_exp_f32_e64 v187, v187\n"
    "v_mfma_f32_16x16x32_bf16 v[56:59], v[116:119], v[104:107], v[56:59]\n"
    "v_exp_f32_e64 v188, v188\n"
    "v_mfma_f32_16x16x32_bf16 v[60:63], v[116:119], v[108:111], v[60:63]\n"
    "v_exp_f32_e64 v189, v189\n"
    "v_mfma_f32_16x16x32_bf16 v[48:51], v[152:155], v[144:147], v[48:51]\n"
    "v_exp_f32_e64 v190, v190\n"
    "v_mfma_f32_16x16x32_bf16 v[52:55], v[152:155], v[148:151], v[52:55]\n"
    "v_exp_f32_e64 v191, v191\n"
    "v_mfma_f32_16x16x32_bf16 v[56:59], v[156:159], v[144:147], v[56:59]\n"
    "v_mul_f32 v8, v0, v8\n"
    "v_mul_f32 v9, v1, v9\n"
    "v_mfma_f32_16x16x32_bf16 v[60:63], v[156:159], v[148:151], v[60:63]\n"
    "v_mul_f32 v10, v2, v10\n"
    "v_mul_f32 v11, v3, v11\n"
    "v_mfma_f32_16x16x32_bf16 v[64:67], v[120:123], v[104:107], v[64:67]\n"
    "v_mul_f32 v12, v4, v12\n"
    "v_mul_f32 v13, v5, v13\n"
    "v_mfma_f32_16x16x32_bf16 v[68:71], v[120:123], v[108:111], v[68:71]\n"
    "v_mul_f32 v14, v6, v14\n"
    "v_mul_f32 v15, v7, v15\n"
    "v_mfma_f32_16x16x32_bf16 v[72:75], v[124:127], v[104:107], v[72:75]\n"
    "v_add_f32 v184, 1.0, v184\n"
    "v_add_f32 v185, 1.0, v185\n"
    "v_mfma_f32_16x16x32_bf16 v[76:79], v[124:127], v[108:111], v[76:79]\n"
    "v_add_f32 v186, 1.0, v186\n"
    "v_add_f32 v187, 1.0, v187\n"
    "v_mfma_f32_16x16x32_bf16 v[64:67], v[160:163], v[144:147], v[64:67]\n"
    "v_add_f32 v188, 1.0, v188\n"
    "v_add_f32 v189, 1.0, v189\n"
    "v_mfma_f32_16x16x32_bf16 v[68:71], v[160:163], v[148:151], v[68:71]\n"
    "v_add_f32 v190, 1.0, v190\n"
    "v_add_f32 v191, 1.0, v191\n"
    "v_mfma_f32_16x16x32_bf16 v[72:75], v[164:167], v[144:147], v[72:75]\n"
    "v_rcp_f32_e64 v184, v184\n"
    "v_mfma_f32_16x16x32_bf16 v[76:79], v[164:167], v[148:151], v[76:79]\n"
    "v_rcp_f32_e64 v185, v185\n"
    "v_mfma_f32_16x16x32_bf16 v[80:83], v[128:131], v[104:107], v[80:83]\n"
    "v_rcp_f32_e64 v186, v186\n"
    "v_mfma_f32_16x16x32_bf16 v[84:87], v[128:131], v[108:111], v[84:87]\n"
    "v_rcp_f32_e64 v187, v187\n"
    "v_mfma_f32_16x16x32_bf16 v[88:91], v[132:135], v[104:107], v[88:91]\n"
    "v_rcp_f32_e64 v188, v188\n"
    "v_mfma_f32_16x16x32_bf16 v[92:95], v[132:135], v[108:111], v[92:95]\n"
    "v_rcp_f32_e64 v189, v189\n"
    "v_mfma_f32_16x16x32_bf16 v[80:83], v[168:171], v[144:147], v[80:83]\n"
    "v_rcp_f32_e64 v190, v190\n"
    "v_mfma_f32_16x16x32_bf16 v[84:87], v[168:171], v[148:151], v[84:87]\n"
    "v_rcp_f32_e64 v191, v191\n"
    "v_mfma_f32_16x16x32_bf16 v[88:91], v[172:175], v[144:147], v[88:91]\n"
    "v_mul_f32 v8, v8, v184\n"
    "v_mul_f32 v9, v9, v185\n"
    "v_mfma_f32_16x16x32_bf16 v[92:95], v[172:175], v[148:151], v[92:95]\n"
    "v_mul_f32 v10, v10, v186\n"
    "v_mul_f32 v11, v11, v187\n"
    "v_mul_f32 v12, v12, v188\n"
    "v_mul_f32 v13, v13, v189\n"
    "v_mul_f32 v14, v14, v190\n"
    "v_mul_f32 v15, v15, v191\n"
    "v_cvt_pk_bf16_f32 v0, v8, v9\n"
    "v_cvt_pk_bf16_f32 v2, v12, v13\n"
    "v_cvt_pk_bf16_f32 v1, v10, v11\n"
    "v_cvt_pk_bf16_f32 v3, v14, v15\n"
    "v_pk_mul_f32 v[184:185], v[16:17], v[16:17]\n"
    "v_permlane16_swap_b32 v0, v2\n"
    "v_permlane16_swap_b32 v1, v3\n"
    "buffer_store_dwordx4 v[0:3], v240, s[20:23], s66 offen sc1\n"
    "v_pk_mul_f32 v[186:187], v[18:19], v[18:19]\n"
    "v_pk_mul_f32 v[188:189], v[20:21], v[20:21]\n"
    "v_pk_mul_f32 v[190:191], v[22:23], v[22:23]\n"
    "v_pk_fma_f32 v[184:185], v[184:185], v[246:247], v[246:247] op_sel:[0,0,1] op_sel_hi:[1,0,1]\n"
    "v_pk_fma_f32 v[186:187], v[186:187], v[246:247], v[246:247] op_sel:[0,0,1] op_sel_hi:[1,0,1]\n"
    "v_pk_fma_f32 v[188:189], v[188:189], v[246:247], v[246:247] op_sel:[0,0,1] op_sel_hi:[1,0,1]\n"
    "v_pk_fma_f32 v[190:191], v[190:191], v[246:247], v[246:247] op_sel:[0,0,1] op_sel_hi:[1,0,1]\n"
    "v_pk_mul_f32 v[184:185], v[184:185], v[16:17]\n"
    "v_pk_mul_f32 v[186:187], v[186:187], v[18:19]\n"
    "v_pk_mul_f32 v[188:189], v[188:189], v[20:21]\n"
    "v_pk_mul_f32 v[190:191], v[190:191], v[22:23]\n"
    "v_exp_f32 v184, v184\n"
    "v_exp_f32 v185, v185\n"
    "v_pk_mul_f32 v[24:25], v[16:17], v[24:25]\n"
    "v_exp_f32 v186, v186\n"
    "v_exp_f32 v187, v187\n"
    "v_pk_mul_f32 v[26:27], v[18:19], v[26:27]\n"
    "v_exp_f32 v188, v188\n"
    "v_exp_f32 v189, v189\n"
    "v_pk_mul_f32 v[28:29], v[20:21], v[28:29]\n"
    "v_exp_f32 v190, v190\n"
    "v_exp_f32 v191, v191\n"
    "v_pk_mul_f32 v[30:31], v[22:23], v[30:31]\n"
    "v_pk_add_f32 v[184:185], v[184:185], 1.0 op_sel_hi:[1,0]\n"
    "v_pk_add_f32 v[186:187], v[186:187], 1.0 op_sel_hi:[1,0]\n"
    "v_pk_add_f32 v[188:189], v[188:189], 1.0 op_sel_hi:[1,0]\n"
    "v_pk_add_f32 v[190:191], v[190:191], 1.0 op_sel_hi:[1,0]\n"
    "v_rcp_f32 v184, v184\n"
    "v_rcp_f32 v185, v185\n"
    "v_rcp_f32 v186, v186\n"
    "v_rcp_f32 v187, v187\n"
    "v_rcp_f32 v188, v188\n"
    "v_rcp_f32 v189, v189\n"
    "v_rcp_f32 v190, v190\n"
    "v_rcp_f32 v191, v191\n"
    "v_pk_mul_f32 v[24:25], v[24:25], v[184:185]\n"
    "v_pk_mul_f32 v[26:27], v[26:27], v[186:187]\n"
    "v_pk_mul_f32 v[28:29], v[28:29], v[188:189]\n"
    "v_pk_mul_f32 v[30:31], v[30:31], v[190:191]\n"
    "v_cvt_pk_bf16_f32 v16, v24, v25\n"
    "v_cvt_pk_bf16_f32 v18, v28, v29\n"
    "v_cvt_pk_bf16_f32 v17, v26, v27\n"
    "v_cvt_pk_bf16_f32 v19, v30, v31\n"
    "v_pk_mul_f32 v[184:185], v[32:33], v[32:33]\n"
    "v_permlane16_swap_b32 v16, v18\n"
    "v_permlane16_swap_b32 v17, v19\n"
    "buffer_store_dwordx4 v[16:19], v240, s[20:23], s67 offen sc1\n"
    "v_pk_mul_f32 v[186:187], v[34:35], v[34:35]\n"
    "v_pk_mul_f32 v[188:189], v[36:37], v[36:37]\n"
    "v_pk_mul_f32 v[190:191], v[38:39], v[38:39]\n"
    "v_pk_fma_f32 v[184:185], v[184:185], v[246:247], v[246:247] op_sel:[0,0,1] op_sel_hi:[1,0,1]\n"
    "v_pk_fma_f32 v[186:187], v[186:187], v[246:247], v[246:247] op_sel:[0,0,1] op_sel_hi:[1,0,1]\n"
    "v_pk_fma_f32 v[188:189], v[188:189], v[246:247], v[246:247] op_sel:[0,0,1] op_sel_hi:[1,0,1]\n"
    "v_pk_fma_f32 v[190:191], v[190:191], v[246:247], v[246:247] op_sel:[0,0,1] op_sel_hi:[1,0,1]\n"
    "v_pk_mul_f32 v[184:185], v[184:185], v[32:33]\n"
    "v_pk_mul_f32 v[186:187], v[186:187], v[34:35]\n"
    "v_pk_mul_f32 v[188:189], v[188:189], v[36:37]\n"
    "v_pk_mul_f32 v[190:191], v[190:191], v[38:39]\n"
    "v_exp_f32 v184, v184\n"
    "v_exp_f32 v185, v185\n"
    "v_pk_mul_f32 v[40:41], v[32:33], v[40:41]\n"
    "v_exp_f32 v186, v186\n"
    "v_exp_f32 v187, v187\n"
    "v_pk_mul_f32 v[42:43], v[34:35], v[42:43]\n"
    "v_exp_f32 v188, v188\n"
    "v_exp_f32 v189, v189\n"
    "v_pk_mul_f32 v[44:45], v[36:37], v[44:45]\n"
    "v_exp_f32 v190, v190\n"
    "v_exp_f32 v191, v191\n"
    "v_pk_mul_f32 v[46:47], v[38:39], v[46:47]\n"
    "v_pk_add_f32 v[184:185], v[184:185], 1.0 op_sel_hi:[1,0]\n"
    "v_pk_add_f32 v[186:187], v[186:187], 1.0 op_sel_hi:[1,0]\n"
    "v_pk_add_f32 v[188:189], v[188:189], 1.0 op_sel_hi:[1,0]\n"
    "v_pk_add_f32 v[190:191], v[190:191], 1.0 op_sel_hi:[1,0]\n"
    "v_rcp_f32 v184, v184\n"
    "v_rcp_f32 v185, v185\n"
    "v_rcp_f32 v186, v186\n"
    "v_rcp_f32 v187, v187\n"
    "v_rcp_f32 v188, v188\n"
    "v_rcp_f32 v189, v189\n"
    "v_rcp_f32 v190, v190\n"
    "v_rcp_f32 v191, v191\n"
    "v_pk_mul_f32 v[40:41], v[40:41], v[184:185]\n"
    "v_pk_mul_f32 v[42:43], v[42:43], v[186:187]\n"
    "v_pk_mul_f32 v[44:45], v[44:45], v[188:189]\n"
    "v_pk_mul_f32 v[46:47], v[46:47], v[190:191]\n"
    "v_cvt_pk_bf16_f32 v32, v40, v41\n"
    "v_cvt_pk_bf16_f32 v34, v44, v45\n"
    "v_cvt_pk_bf16_f32 v33, v42, v43\n"
    "v_cvt_pk_bf16_f32 v35, v46, v47\n"
    "v_pk_mul_f32 v[184:185], v[48:49], v[48:49]\n"
    "v_permlane16_swap_b32 v32, v34\n"
    "v_permlane16_swap_b32 v33, v35\n"
    "buffer_store_dwordx4 v[32:35], v240, s[20:23], s68 offen sc1\n"
    "v_pk_mul_f32 v[186:187], v[50:51], v[50:51]\n"
    "v_pk_mul_f32 v[188:189], v[52:53], v[52:53]\n"
    "v_pk_mul_f32 v[190:191], v[54:55], v[54:55]\n"
    "v_pk_fma_f32 v[184:185], v[184:185], v[246:247], v[246:247] op_sel:[0,0,1] op_sel_hi:[1,0,1]\n"
    "v_pk_fma_f32 v[186:187], v[186:187], v[246:247], v[246:247] op_sel:[0,0,1] op_sel_hi:[1,0,1]\n"
    "v_pk_fma_f32 v[188:189], v[188:189], v[246:247], v[246:247] op_sel:[0,0,1] op_sel_hi:[1,0,1]\n"
    "v_pk_fma_f32 v[190:191], v[190:191], v[246:247], v[246:247] op_sel:[0,0,1] op_sel_hi:[1,0,1]\n"
    "v_pk_mul_f32 v[184:185], v[184:185], v[48:49]\n"
    "v_pk_mul_f32 v[186:187], v[186:187], v[50:51]\n"
    "v_pk_mul_f32 v[188:189], v[188:189], v[52:53]\n"
    "v_pk_mul_f32 v[190:191], v[190:191], v[54:55]\n"
    "v_exp_f32 v184, v184\n"
    "v_exp_f32 v185, v185\n"
    "v_pk_mul_f32 v[56:57], v[48:49], v[56:57]\n"
    "v_exp_f32 v186, v186\n"
    "v_exp_f32 v187, v187\n"
    "v_pk_mul_f32 v[58:59], v[50:51], v[58:59]\n"
    "v_exp_f32 v188, v188\n"
    "v_exp_f32 v189, v189\n"
    "v_pk_mul_f32 v[60:61], v[52:53], v[60:61]\n"
    "v_exp_f32 v190, v190\n"
    "v_exp_f32 v191, v191\n"
    "v_pk_mul_f32 v[62:63], v[54:55], v[62:63]\n"
    "v_pk_add_f32 v[184:185], v[184:185], 1.0 op_sel_hi:[1,0]\n"
    "v_pk_add_f32 v[186:187], v[186:187], 1.0 op_sel_hi:[1,0]\n"
    "v_pk_add_f32 v[188:189], v[188:189], 1.0 op_sel_hi:[1,0]\n"
    "v_pk_add_f32 v[190:191], v[190:191], 1.0 op_sel_hi:[1,0]\n"
    "v_rcp_f32 v184, v184\n"
    "v_rcp_f32 v185, v185\n"
    "v_rcp_f32 v186, v186\n"
    "v_rcp_f32 v187, v187\n"
    "v_rcp_f32 v188, v188\n"
    "v_rcp_f32 v189, v189\n"
    "v_rcp_f32 v190, v190\n"
    "v_rcp_f32 v191, v191\n"
    "v_pk_mul_f32 v[56:57], v[56:57], v[184:185]\n"
    "v_pk_mul_f32 v[58:59], v[58:59], v[186:187]\n"
    "v_pk_mul_f32 v[60:61], v[60:61], v[188:189]\n"
    "v_pk_mul_f32 v[62:63], v[62:63], v[190:191]\n"
    "v_cvt_pk_bf16_f32 v176, v56, v57\n"
    "v_cvt_pk_bf16_f32 v178, v60, v61\n"
    "v_cvt_pk_bf16_f32 v177, v58, v59\n"
    "v_cvt_pk_bf16_f32 v179, v62, v63\n"
    "v_pk_mul_f32 v[184:185], v[64:65], v[64:65]\n"
    "v_permlane16_swap_b32 v176, v178\n"
    "v_permlane16_swap_b32 v177, v179\n"
    "buffer_store_dwordx4 v[176:179], v240, s[20:23], s69 offen sc1\n"
    "v_pk_mul_f32 v[186:187], v[66:67], v[66:67]\n"
    "v_pk_mul_f32 v[188:189], v[68:69], v[68:69]\n"
    "v_pk_mul_f32 v[190:191], v[70:71], v[70:71]\n"
    "v_pk_fma_f32 v[184:185], v[184:185], v[246:247], v[246:247] op_sel:[0,0,1] op_sel_hi:[1,0,1]\n"
    "v_pk_fma_f32 v[186:187], v[186:187], v[246:247], v[246:247] op_sel:[0,0,1] op_sel_hi:[1,0,1]\n"
    "v_pk_fma_f32 v[188:189], v[188:189], v[246:247], v[246:247] op_sel:[0,0,1] op_sel_hi:[1,0,1]\n"
    "v_pk_fma_f32 v[190:191], v[190:191], v[246:247], v[246:247] op_sel:[0,0,1] op_sel_hi:[1,0,1]\n"
    "v_pk_mul_f32 v[184:185], v[184:185], v[64:65]\n"
    "v_pk_mul_f32 v[186:187], v[186:187], v[66:67]\n"
    "v_pk_mul_f32 v[188:189], v[188:189], v[68:69]\n"
    "v_pk_mul_f32 v[190:191], v[190:191], v[70:71]\n"
    "v_exp_f32 v184, v184\n"
    "v_exp_f32 v185, v185\n"
    "v_pk_mul_f32 v[72:73], v[64:65], v[72:73]\n"
    "v_exp_f32 v186, v186\n"
    "v_exp_f32 v187, v187\n"
    "v_pk_mul_f32 v[74:75], v[66:67], v[74:75]\n"
    "v_exp_f32 v188, v188\n"
    "v_exp_f32 v189, v189\n"
    "v_pk_mul_f32 v[76:77], v[68:69], v[76:77]\n"
    "v_exp_f32 v190, v190\n"
    "v_exp_f32 v191, v191\n"
    "v_pk_mul_f32 v[78:79], v[70:71], v[78:79]\n"
    "v_pk_add_f32 v[184:185], v[184:185], 1.0 op_sel_hi:[1,0]\n"
    "v_pk_add_f32 v[186:187], v[186:187], 1.0 op_sel_hi:[1,0]\n"
    "v_pk_add_f32 v[188:189], v[188:189], 1.0 op_sel_hi:[1,0]\n"
    "v_pk_add_f32 v[190:191], v[190:191], 1.0 op_sel_hi:[1,0]\n"
    "v_rcp_f32 v184, v184\n"
    "v_rcp_f32 v185, v185\n"
    "v_rcp_f32 v186, v186\n"
    "v_rcp_f32 v187, v187\n"
    "v_rcp_f32 v188, v188\n"
    "v_rcp_f32 v189, v189\n"
    "v_rcp_f32 v190, v190\n"
    "v_rcp_f32 v191, v191\n"
    "v_pk_mul_f32 v[72:73], v[72:73], v[184:185]\n"
    "v_pk_mul_f32 v[74:75], v[74:75], v[186:187]\n"
    "v_pk_mul_f32 v[76:77], v[76:77], v[188:189]\n"
    "v_pk_mul_f32 v[78:79], v[78:79], v[190:191]\n"
    "v_cvt_pk_bf16_f32 v232, v72, v73\n"
    "v_cvt_pk_bf16_f32 v234, v76, v77\n"
    "v_cvt_pk_bf16_f32 v233, v74, v75\n"
    "v_cvt_pk_bf16_f32 v235, v78, v79\n"
    "v_pk_mul_f32 v[184:185], v[80:81], v[80:81]\n"
    "v_permlane16_swap_b32 v232, v234\n"
    "v_permlane16_swap_b32 v233, v235\n"
    "buffer_store_dwordx4 v[232:235], v240, s[20:23], s70 offen sc1\n"
    "v_pk_mul_f32 v[186:187], v[82:83], v[82:83]\n"
    "v_pk_mul_f32 v[188:189], v[84:85], v[84:85]\n"
    "v_pk_mul_f32 v[190:191], v[86:87], v[86:87]\n"
    "v_pk_fma_f32 v[184:185], v[184:185], v[246:247], v[246:247] op_sel:[0,0,1] op_sel_hi:[1,0,1]\n"
    "v_pk_fma_f32 v[186:187], v[186:187], v[246:247], v[246:247] op_sel:[0,0,1] op_sel_hi:[1,0,1]\n"
    "v_pk_fma_f32 v[188:189], v[188:189], v[246:247], v[246:247] op_sel:[0,0,1] op_sel_hi:[1,0,1]\n"
    "v_pk_fma_f32 v[190:191], v[190:191], v[246:247], v[246:247] op_sel:[0,0,1] op_sel_hi:[1,0,1]\n"
    "v_pk_mul_f32 v[184:185], v[184:185], v[80:81]\n"
    "v_pk_mul_f32 v[186:187], v[186:187], v[82:83]\n"
    "v_pk_mul_f32 v[188:189], v[188:189], v[84:85]\n"
    "v_pk_mul_f32 v[190:191], v[190:191], v[86:87]\n"
    "v_exp_f32 v184, v184\n"
    "v_exp_f32 v185, v185\n"
    "v_pk_mul_f32 v[88:89], v[80:81], v[88:89]\n"
    "v_exp_f32 v186, v186\n"
    "v_exp_f32 v187, v187\n"
    "v_pk_mul_f32 v[90:91], v[82:83], v[90:91]\n"
    "v_exp_f32 v188, v188\n"
    "v_exp_f32 v189, v189\n"
    "v_pk_mul_f32 v[92:93], v[84:85], v[92:93]\n"
    "v_exp_f32 v190, v190\n"
    "v_exp_f32 v191, v191\n"
    "v_pk_mul_f32 v[94:95], v[86:87], v[94:95]\n"
    "v_pk_add_f32 v[184:185], v[184:185], 1.0 op_sel_hi:[1,0]\n"
    "v_pk_add_f32 v[186:187], v[186:187], 1.0 op_sel_hi:[1,0]\n"
    "v_pk_add_f32 v[188:189], v[188:189], 1.0 op_sel_hi:[1,0]\n"
    "v_pk_add_f32 v[190:191], v[190:191], 1.0 op_sel_hi:[1,0]\n"
    "v_rcp_f32 v184, v184\n"
    "v_rcp_f32 v185, v185\n"
    "v_rcp_f32 v186, v186\n"
    "v_rcp_f32 v187, v187\n"
    "v_rcp_f32 v188, v188\n"
    "v_rcp_f32 v189, v189\n"
    "v_rcp_f32 v190, v190\n"
    "v_rcp_f32 v191, v191\n"
    "v_pk_mul_f32 v[88:89], v[88:89], v[184:185]\n"
    "v_pk_mul_f32 v[90:91], v[90:91], v[186:187]\n"
    "v_pk_mul_f32 v[92:93], v[92:93], v[188:189]\n"
    "v_pk_mul_f32 v[94:95], v[94:95], v[190:191]\n"
    "v_cvt_pk_bf16_f32 v236, v88, v89\n"
    "v_cvt_pk_bf16_f32 v238, v92, v93\n"
    "v_cvt_pk_bf16_f32 v237, v90, v91\n"
    "v_cvt_pk_bf16_f32 v239, v94, v95\n"
    "v_permlane16_swap_b32 v236, v238\n"
    "s_nop 0\n"
    "v_permlane16_swap_b32 v237, v239\n"
    "buffer_store_dwordx4 v[236:239], v240, s[20:23], s71 offen sc1\n"
    "s_setprio 0\n"
    "s_cmp_lg_u32 s64, 0\n"
    "s_cbranch_scc1 .LY26_%=\n"
    "ds_read_b64 v[80:81], v248 offset:0\n"
    "ds_read_b64 v[82:83], v248 offset:8\n"
    "ds_read_b64_tr_b16 v[84:85], v249 offset:64\n"
    "ds_read_b64_tr_b16 v[86:87], v249 offset:0\n"
    "ds_read_b64 v[88:89], v248 offset:32\n"
    "ds_read_b64 v[90:91], v248 offset:40\n"
    "s_waitcnt lgkmcnt(2)\n"
    "v_mfma_f32_16x16x32_bf16 v[0:3], v[80:83], v[84:87], 0\n"
    "ds_read_b64 v[92:93], v248 offset:64\n"
    "ds_read_b64 v[94:95], v248 offset:72\n"
    "ds_read_b64_tr_b16 v[96:97], v249 offset:576\n"
    "s_waitcnt lgkmcnt(3)\n"
    "v_mfma_f32_16x16x32_bf16 v[4:7], v[88:91], v[84:87], 0\n"
    "ds_read_b64_tr_b16 v[98:99], v249 offset:512\n"
    "ds_read_b64 v[100:101], v248 offset:96\n"
    "ds_read_b64 v[102:103], v248 offset:104\n"
    "s_waitcnt lgkmcnt(4)\n"
    "v_mfma_f32_16x16x32_bf16 v[8:11], v[92:95], v[84:87], 0\n"
    "ds_read_b64 v[104:105], v248 offset:128\n"
    "ds_read_b64 v[106:107], v248 offset:136\n"
    "s_waitcnt lgkmcnt(4)\n"
    "v_mfma_f32_16x16x32_bf16 v[8:11], v[80:83], v[96:99], v[8:11]\n"
    "ds_read_b64_tr_b16 v[108:109], v249 offset:1088\n"
    "ds_read_b64_tr_b16 v[110:111], v249 offset:1024\n"
    "s_waitcnt lgkmcnt(4)\n"
    "v_mfma_f32_16x16x32_bf16 v[12:15], v[100:103], v[84:87], 0\n"
    "ds_read_b64 v[112:113], v248 offset:160\n"
    "ds_read_b64 v[114:115], v248 offset:168\n"
    "v_mfma_f32_16x16x32_bf16 v[12:15], v[88:91], v[96:99], v[12:15]\n"
    "ds_read_b64 v[116:117], v248 offset:192\n"
    "ds_read_b64 v[118:119], v248 offset:200\n"
    "s_waitcnt lgkmcnt(6)\n"
    "v_mfma_f32_16x16x32_bf16 v[16:19], v[104:107], v[84:87], 0\n"
    "v_add_f32_dpp v2, v3, v2 wave_shr:1 row_mask:0xf bank_mask:0xf\n"
    "ds_read_b64_tr_b16 v[120:121], v249 offset:1600\n"
    "v_mfma_f32_16x16x32_bf16 v[16:19], v[92:95], v[96:99], v[16:19]\n"
    "ds_read_b64_tr_b16 v[122:123], v249 offset:1536\n"
    "v_add_f32_dpp v1, v2, v1 wave_shr:1 row_mask:0xf bank_mask:0xf\n"
    "s_waitcnt lgkmcnt(6)\n"
    "v_mfma_f32_16x16x32_bf16 v[16:19], v[80:83], v[108:111], v[16:19]\n"
    "ds_read_b64 v[124:125], v248 offset:224\n"
    "ds_read_b64 v[126:127], v248 offset:232\n"
    "s_waitcnt lgkmcnt(6)\n"
    "v_mfma_f32_16x16x32_bf16 v[20:23], v[112:115], v[84:87], 0\n"
    "v_add_f32_dpp v0, v1, v0 wave_shr:1 row_mask:0xf bank_mask:0xf\n"
    "v_fmac_f32_dpp v6, v3, v243 wave_ror:1 row_mask:0xf bank_mask:0xf\n"
    "v_mfma_f32_16x16x32_bf16 v[20:23], v[100:103], v[96:99], v[20:23]\n"
    "v_add_f32_dpp v6, v7, v6 wave_shr:1 row_mask:0xf bank_mask:0xf\n"
    "v_fmac_f32_dpp v5, v2, v243 wave_ror:1 row_mask:0xf bank_mask:0xf\n"
    "v_mfma_f32_16x16x32_bf16 v[20:23], v[88:91], v[108:111], v[20:23]\n"
    "v_fmac_f32_dpp v4, v1, v243 wave_ror:1 row_mask:0xf bank_mask:0xf\n"
    "v_add_f32_dpp v5, v6, v5 wave_shr:1 row_mask:0xf bank_mask:0xf\n"
    "s_waitcnt lgkmcnt(4)\n"
    "v_mfma_f32_16x16x32_bf16 v[24:27], v[116:119], v[84:87], 0\n"
    "ds_read_b64_tr_b16 v[128:129], v249 offset:6208\n"
    "ds_read_b64_tr_b16 v[130:131], v249 offset:6144\n"
    "v_mfma_f32_16x16x32_bf16 v[24:27], v[104:107], v[96:99], v[24:27]\n"
    "v_add_f32_dpp v4, v5, v4 wave_shr:1 row_mask:0xf bank_mask:0xf\n"
    "v_cvt_pk_bf16_f32 v64, v0, v4\n"
    "v_mfma_f32_16x16x32_bf16 v[24:27], v[92:95], v[108:111], v[24:27]\n"
    "ds_read_b64_tr_b16 v[132:133], v249 offset:6720\n"
    "ds_read_b64_tr_b16 v[134:135], v249 offset:6656\n"
    "s_waitcnt lgkmcnt(6)\n"
    "v_mfma_f32_16x16x32_bf16 v[24:27], v[80:83], v[120:123], v[24:27]\n"
    "v_fmac_f32_dpp v10, v7, v243 wave_ror:1 row_mask:0xf bank_mask:0xf\n"
    "v_add_f32_dpp v10, v11, v10 wave_shr:1 row_mask:0xf bank_mask:0xf\n"
    "s_waitcnt lgkmcnt(4)\n"
    "v_mfma_f32_16x16x32_bf16 v[28:31], v[124:127], v[84:87], 0\n"
    "ds_read_b64_tr_b16 v[136:137], v249 offset:7232\n"
    "ds_read_b64_tr_b16 v[138:139], v249 offset:7168\n"
    "v_mfma_f32_16x16x32_bf16 v[28:31], v[112:115], v[96:99], v[28:31]\n"
    "v_fmac_f32_dpp v9, v6, v243 wave_ror:1 row_mask:0xf bank_mask:0xf\n"
    "v_fmac_f32_dpp v8, v5, v243 wave_ror:1 row_mask:0xf bank_mask:0xf\n"
    "v_mfma_f32_16x16x32_bf16 v[28:31], v[100:103], v[108:111], v[28:31]\n"
    "ds_read_b64_tr_b16 v[140:141], v249 offset:7744\n"
    "ds_read_b64_tr_b16 v[142:143], v249 offset:7680\n"
    "v_mfma_f32_16x16x32_bf16 v[28:31], v[88:91], v[120:123], v[28:31]\n"
    "v_add_f32_dpp v9, v10, v9 wave_shr:1 row_mask:0xf bank_mask:0xf\n"
    "ds_read_b64_tr_b16 v[144:145], v249 offset:5696\n"
    "s_waitcnt lgkmcnt(7)\n"
    "v_mfma_f32_16x16x32_bf16 v[32:35], v[80:83], v[128:131], 0\n"
    "ds_read_b64_tr_b16 v[146:147], v249 offset:5632\n"
    "v_add_f32_dpp v8, v9, v8 wave_shr:1 row_mask:0xf bank_mask:0xf\n"
    "v_mfma_f32_16x16x32_bf16 v[36:39], v[88:91], v[128:131], 0\n"
    "v_fmac_f32_dpp v14, v11, v243 wave_ror:1 row_mask:0xf bank_mask:0xf\n"
    "v_add_f32_dpp v14, v15, v14 wave_shr:1 row_mask:0xf bank_mask:0xf\n"
    "s_waitcnt lgkmcnt(6)\n"
    "v_mfma_f32_16x16x32_bf16 v[40:43], v[80:83], v[132:135], 0\n"
    "v_fmac_f32_dpp v13, v10, v243 wave_ror:1 row_mask:0xf bank_mask:0xf\n"
    "v_fmac_f32_dpp v12, v9, v243 wave_ror:1 row_mask:0xf bank_mask:0xf\n"
    "v_mfma_f32_16x16x32_bf16 v[44:47], v[88:91], v[132:135], 0\n"
    "v_add_f32_dpp v13, v14, v13 wave_shr:1 row_mask:0xf bank_mask:0xf\n"
    "ds_read_b64_tr_b16 v[148:149], v249 offset:5184\n"
    "s_waitcnt lgkmcnt(5)\n"
    "v_mfma_f32_16x16x32_bf16 v[48:51], v[80:83], v[136:139], 0\n"
    "v_add_f32_dpp v12, v13, v12 wave_shr:1 row_mask:0xf bank_mask:0xf\n"
    "v_cvt_pk_bf16_f32 v65, v8, v12\n"
    "v_mfma_f32_16x16x32_bf16 v[52:55], v[88:91], v[136:139], 0\n"
    "v_fmac_f32_dpp v18, v15, v243 wave_ror:1 row_mask:0xf bank_mask:0xf\n"
    "v_add_f32_dpp v18, v19, v18 wave_shr:1 row_mask:0xf bank_mask:0xf\n"
    "s_waitcnt lgkmcnt(3)\n"
    "v_mfma_f32_16x16x32_bf16 v[56:59], v[80:83], v[140:143], 0\n"
    "v_fmac_f32_dpp v17, v14, v243 wave_ror:1 row_mask:0xf bank_mask:0xf\n"
    "v_fmac_f32_dpp v16, v13, v243 wave_ror:1 row_mask:0xf bank_mask:0xf\n"
    "v_mfma_f32_16x16x32_bf16 v[60:63], v[88:91], v[140:143], 0\n"
    "v_add_f32_dpp v17, v18, v17 wave_shr:1 row_mask:0xf bank_mask:0xf\n"
    "ds_read_b64_tr_b16 v[150:151], v249 offset:5120\n"
    "s_waitcnt lgkmcnt(2)\n"
    "v_mfma_f32_16x16x32_bf16 v[32:35], v[92:95], v[144:147], v[32:35]\n"
    "v_add_f32_dpp v16, v17, v16 wave_shr:1 row_mask:0xf bank_mask:0xf\n"
    "v_fmac_f32_dpp v22, v19, v243 wave_ror:1 row_mask:0xf bank_mask:0xf\n"
    "v_mfma_f32_16x16x32_bf16 v[36:39], v[100:103], v[144:147], v[36:39]\n"
    "v_add_f32_dpp v22, v23, v22 wave_shr:1 row_mask:0xf bank_mask:0xf\n"
    "v_fmac_f32_dpp v21, v18, v243 wave_ror:1 row_mask:0xf bank_mask:0xf\n"
    "v_mfma_f32_16x16x32_bf16 v[40:43], v[92:95], v[128:131], v[40:43]\n"
    "v_fmac_f32_dpp v20, v17, v243 wave_ror:1 row_mask:0xf bank_mask:0xf\n"
    "v_add_f32_dpp v21, v22, v21 wave_shr:1 row_mask:0xf bank_mask:0xf\n"
    "v_mfma_f32_16x16x32_bf16 v[44:47], v[100:103], v[128:131], v[44:47]\n"
    "ds_read_b64_tr_b16 v[152:153], v249 offset:4672\n"
    "ds_read_b64_tr_b16 v[154:155], v249 offset:4608\n"
    "v_mfma_f32_16x16x32_bf16 v[48:51], v[92:95], v[132:135], v[48:51]\n"
    "v_add_f32_dpp v20, v21, v20 wave_shr:1 row_mask:0xf bank_mask:0xf\n"
    "v_cvt_pk_bf16_f32 v66, v16, v20\n"
    "v_mfma_f32_16x16x32_bf16 v[52:55], v[100:103], v[132:135], v[52:55]\n"
    "v_fmac_f32_dpp v26, v23, v243 wave_ror:1 row_mask:0xf bank_mask:0xf\n"
    "v_add_f32_dpp v26, v27, v26 wave_shr:1 row_mask:0xf bank_mask:0xf\n"
    "v_mfma_f32_16x16x32_bf16 v[56:59], v[92:95], v[136:139], v[56:59]\n"
    "v_fmac_f32_dpp v25, v22, v243 wave_ror:1 row_mask:0xf bank_mask:0xf\n"
    "v_fmac_f32_dpp v24, v21, v243 wave_ror:1 row_mask:0xf bank_mask:0xf\n"
    "v_mfma_f32_16x16x32_bf16 v[60:63], v[100:103], v[136:139], v[60:63]\n"
    "v_add_f32_dpp v25, v26, v25 wave_shr:1 row_mask:0xf bank_mask:0xf\n"
    "ds_read_b64 v[156:157], v248 offset:256\n"
    "s_waitcnt lgkmcnt(3)\n"
    "s_nop 0\n"
    "v_mfma_f32_16x16x32_bf16 v[32:35], v[104:107], v[148:151], v[32:35]\n"
    "v_add_f32_dpp v24, v25, v24 wave_shr:1 row_mask:0xf bank_mask:0xf\n"
    "v_fmac_f32_dpp v30, v27, v243 wave_ror:1 row_mask:0xf bank_mask:0xf\n"
    "v_mfma_f32_16x16x32_bf16 v[36:39], v[112:115], v[148:151], v[36:39]\n"
    "v_add_f32_dpp v30, v31, v30 wave_shr:1 row_mask:0xf bank_mask:0xf\n"
    "v_fmac_f32_dpp v29, v26, v243 wave_ror:1 row_mask:0xf bank_mask:0xf\n"
    "v_mfma_f32_16x16x32_bf16 v[40:43], v[104:107], v[144:147], v[40:43]\n"
    "v_fmac_f32_dpp v28, v25, v243 wave_ror:1 row_mask:0xf bank_mask:0xf\n"
    "v_add_f32_dpp v29, v30, v29 wave_shr:1 row_mask:0xf bank_mask:0xf\n"
    "v_mfma_f32_16x16x32_bf16 v[44:47], v[112:115], v[144:147], v[44:47]\n"
    "ds_read_b64 v[158:159], v248 offset:264\n"
    "ds_read_b64_tr_b16 v[160:161], v249 offset:4160\n"
    "v_mfma_f32_16x16x32_bf16 v[48:51], v[104:107], v[128:131], v[48:51]\n"
    "v_add_f32_dpp v28, v29, v28 wave_shr:1 row_mask:0xf bank_mask:0xf\n"
    "v_cvt_pk_bf16_f32 v67, v24, v28\n"
    "v_mfma_f32_16x16x32_bf16 v[52:55], v[112:115], v[128:131], v[52:55]\n"
    "ds_read_b64_tr_b16 v[162:163], v249 offset:4096\n"
    "ds_read_b64 v[164:165], v248 offset:288\n"
    "v_mfma_f32_16x16x32_bf16 v[56:59], v[104:107], v[132:135], v[56:59]\n"
    "ds_read_b64 v[166:167], v248 offset:296\n"
    "ds_read_b64 v[168:169], v248 offset:320\n"
    "v_mfma_f32_16x16x32_bf16 v[60:63], v[112:115], v[132:135], v[60:63]\n"
    "ds_read_b64 v[170:171], v248 offset:328\n"
    "ds_read_b64_tr_b16 v[172:173], v249 offset:3648\n"
    "s_waitcnt lgkmcnt(9)\n"
    "v_mfma_f32_16x16x32_bf16 v[32:35], v[116:119], v[152:155], v[32:35]\n"
    "ds_read_b64_tr_b16 v[174:175], v249 offset:3584\n"
    "ds_read_b64 v[180:181], v248 offset:352\n"
    "v_mfma_f32_16x16x32_bf16 v[36:39], v[124:127], v[152:155], v[36:39]\n"
    "ds_read_b64 v[182:183], v248 offset:360\n"
    "ds_read_b64 v[184:185], v248 offset:384\n"
    "s_waitcnt vmcnt(0)\n"
    "s_mov_b64 exec, 1\n"
    "buffer_store_dwordx2 v[244:245], off, s[20:23], s72 sc1\n"
    "s_mov_b64 exec, -1\n"
    "v_mfma_f32_16x16x32_bf16 v[40:43], v[116:119], v[148:151], v[40:43]\n"
    "ds_read_b64 v[186:187], v248 offset:392\n"
    "ds_read_b64_tr_b16 v[188:189], v249 offset:3136\n"
    "v_mfma_f32_16x16x32_bf16 v[44:47], v[124:127], v[148:151], v[44:47]\n"
    "ds_read_b64_tr_b16 v[190:191], v249 offset:3072\n"
    "ds_read_b64 v[192:193], v248 offset:416\n"
    "v_mfma_f32_16x16x32_bf16 v[48:51], v[116:119], v[144:147], v[48:51]\n"
    "ds_read_b64 v[194:195], v248 offset:424\n"
    "ds_read_b64 v[196:197], v248 offset:448\n"
    "v_mfma_f32_16x16x32_bf16 v[52:55], v[124:127], v[144:147], v[52:55]\n"
    "ds_read_b64 v[198:199], v248 offset:456\n"
    "ds_read_b64_tr_b16 v[200:201], v249 offset:2624\n"
    "v_mfma_f32_16x16x32_bf16 v[56:59], v[116:119], v[128:131], v[56:59]\n"
    "ds_read_b64_tr_b16 v[202:203], v249 offset:2560\n"
    "ds_read_b64 v[204:205], v248 offset:480\n"
    "v_mfma_f32_16x16x32_bf16 v[60:63], v[124:127], v[128:131], v[60:63]\n"
    "ds_read_b64 v[206:207], v248 offset:488\n"
    "ds_read_b64 v[208:209], v248 offset:512\n"
    "s_waitcnt lgkmcnt(15)\n"
    "v_mfma_f32_16x16x32_bf16 v[32:35], v[156:159], v[160:163], v[32:35]\n"
    "ds_read_b64 v[210:211], v248 offset:520\n"
    "ds_read_b64_tr_b16 v[212:213], v249 offset:2112\n"
    "v_mfma_f32_16x16x32_bf16 v[36:39], v[164:167], v[160:163], v[36:39]\n"
    "ds_read_b64_tr_b16 v[214:215], v249 offset:2048\n"
    "ds_read_b64 v[216:217], v248 offset:576\n"
    "v_mfma_f32_16x16x32_bf16 v[40:43], v[156:159], v[152:155], v[40:43]\n"
    "ds_read_b64 v[218:219], v248 offset:584\n"
    "ds_read_b64 v[220:221], v248 offset:640\n"
    "v_mfma_f32_16x16x32_bf16 v[44:47], v[164:167], v[152:155], v[44:47]\n"
    "ds_read_b64 v[222:223], v248 offset:648\n"
    "ds_read_b64 v[80:81], v248 offset:704\n"
    "v_mfma_f32_16x16x32_bf16 v[48:51], v[156:159], v[148:151], v[48:51]\n"
    "ds_read_b64 v[82:83], v248 offset:712\n"
    "ds_read_b64 v[88:89], v248 offset:768\n"
    "v_mfma_f32_16x16x32_bf16 v[52:55], v[164:167], v[148:151], v[52:55]\n"
    "ds_read_b64 v[90:91], v248 offset:776\n"
    "ds_read_b64 v[140:141], v248 offset:544\n"
    "v_mfma_f32_16x16x32_bf16 v[56:59], v[156:159], v[144:147], v[56:59]\n"
    "ds_read_b64 v[142:143], v248 offset:552\n"
    "ds_read_b64 v[92:93], v248 offset:608\n"
    "v_mfma_f32_16x16x32_bf16 v[60:63], v[164:167], v[144:147], v[60:63]\n"
    "ds_read_b64 v[94:95], v248 offset:616\n"
    "ds_read_b64 v[100:101], v248 offset:672\n"
    "v_mfma_f32_16x16x32_bf16 v[32:35], v[168:171], v[172:175], v[32:35]\n"
    "ds_read_b64 v[102:103], v248 offset:680\n"
    "ds_read_b64 v[136:137], v248 offset:736\n"
    "s_waitcnt lgkmcnt(15)\n"
    "v_mfma_f32_16x16x32_bf16 v[36:39], v[180:183], v[172:175], v[36:39]\n"
    "ds_read_b64 v[138:139], v248 offset:744\n"
    "ds_read_b64 v[104:105], v248 offset:800\n"
    "v_mfma_f32_16x16x32_bf16 v[40:43], v[168:171], v[160:163], v[40:43]\n"
    "ds_read_b64 v[106:107], v248 offset:808\n"
    "ds_read_b64 v[112:113], v248 offset:832\n"
    "v_mfma_f32_16x16x32_bf16 v[44:47], v[180:183], v[160:163], v[44:47]\n"
    "ds_read_b64 v[114:115], v248 offset:840\n"
    "ds_read_b64 v[132:133], v248 offset:864\n"
    "v_mfma_f32_16x16x32_bf16 v[48:51], v[168:171], v[152:155], v[48:51]\n"
    "ds_read_b64 v[134:135], v248 offset:872\n"
    "ds_read_b64 v[116:117], v248 offset:896\n"
    "v_mfma_f32_16x16x32_bf16 v[52:55], v[180:183], v[152:155], v[52:55]\n"
    "ds_read_b64 v[118:119], v248 offset:904\n"
    "ds_read_b64 v[124:125], v248 offset:928\n"
    "v_mfma_f32_16x16x32_bf16 v[56:59], v[168:171], v[148:151], v[56:59]\n"
    "ds_read_b64 v[126:127], v248 offset:936\n"
    "ds_read_b64 v[128:129], v248 offset:960\n"
    "v_mfma_f32_16x16x32_bf16 v[60:63], v[180:183], v[148:151], v[60:63]\n"
    "ds_read_b64 v[130:131], v248 offset:968\n"
    "ds_read_b64 v[156:157], v248 offset:992\n"
    "v_mfma_f32_16x16x32_bf16 v[32:35], v[184:187], v[188:191], v[32:35]\n"
    "ds_read_b64 v[158:159], v248 offset:1000\n"
    "v_mfma_f32_16x16x32_bf16 v[36:39], v[192:195], v[188:191], v[36:39]\n"
    "v_mfma_f32_16x16x32_bf16 v[40:43], v[184:187], v[172:175], v[40:43]\n"
    "v_mfma_f32_16x16x32_bf16 v[44:47], v[192:195], v[172:175], v[44:47]\n"
    "v_mfma_f32_16x16x32_bf16 v[48:51], v[184:187], v[160:163], v[48:51]\n"
    "v_mfma_f32_16x16x32_bf16 v[52:55], v[192:195], v[160:163], v[52:55]\n"
    "v_mfma_f32_16x16x32_bf16 v[56:59], v[184:187], v[152:155], v[56:59]\n"
    "v_mfma_f32_16x16x32_bf16 v[60:63], v[192:195], v[152:155], v[60:63]\n"
    "v_mfma_f32_16x16x32_bf16 v[32:35], v[196:199], v[200:203], v[32:35]\n"
    "v_mfma_f32_16x16x32_bf16 v[36:39], v[204:207], v[200:203], v[36:39]\n"
    "v_mfma_f32_16x16x32_bf16 v[40:43], v[196:199], v[188:191], v[40:43]\n"
    "v_mfma_f32_16x16x32_bf16 v[44:47], v[204:207], v[188:191], v[44:47]\n"
    "v_mfma_f32_16x16x32_bf16 v[48:51], v[196:199], v[172:175], v[48:51]\n"
    "v_mfma_f32_16x16x32_bf16 v[52:55], v[204:207], v[172:175], v[52:55]\n"
    "v_mfma_f32_16x16x32_bf16 v[56:59], v[196:199], v[160:163], v[56:59]\n"
    "v_mfma_f32_16x16x32_bf16 v[60:63], v[204:207], v[160:163], v[60:63]\n"
    "v_mfma_f32_16x16x32_bf16 v[32:35], v[208:211], v[212:215], v[32:35]\n"
    "s_waitcnt lgkmcnt(15)\n"
    "v_mfma_f32_16x16x32_bf16 v[32:35], v[216:219], v[120:123], v[32:35]\n"
    "v_mfma_f32_16x16x32_bf16 v[32:35], v[220:223], v[108:111], v[32:35]\n"
    "v_mfma_f32_16x16x32_bf16 v[32:35], v[80:83], v[96:99], v[32:35]\n"
    "v_mfma_f32_16x16x32_bf16 v[32:35], v[88:91], v[84:87], v[32:35]\n"
    "v_mfma_f32_16x16x32_bf16 v[36:39], v[140:143], v[212:215], v[36:39]\n"
    "v_mfma_f32_16x16x32_bf16 v[36:39], v[92:95], v[120:123], v[36:39]\n"
    "v_mfma_f32_16x16x32_bf16 v[36:39], v[100:103], v[108:111], v[36:39]\n"
    "s_waitcnt lgkmcnt(14)\n"
    "v_mfma_f32_16x16x32_bf16 v[36:39], v[136:139], v[96:99], v[36:39]\n"
    "s_waitcnt lgkmcnt(12)\n"
    "v_mfma_f32_16x16x32_bf16 v[36:39], v[104:107], v[84:87], v[36:39]\n"
    "v_mfma_f32_16x16x32_bf16 v[40:43], v[208:211], v[200:203], v[40:43]\n"
    "v_add_f32_dpp v34, v35, v34 wave_shr:1 row_mask:0xf bank_mask:0xf\n"
    "v_mfma_f32_16x16x32_bf16 v[40:43], v[216:219], v[212:215], v[40:43]\n"
    "v_mfma_f32_16x16x32_bf16 v[40:43], v[220:223], v[120:123], v[40:43]\n"
    "s_mov_b64 exec, s[76:77]\n"
    "ds_write_b128 v253, v[28:31]\n"
    "s_mov_b64 exec, -1\n"
    "v_add_f32_dpp v33, v34, v33 wave_shr:1 row_mask:0xf bank_mask:0xf\n"
    "v_mfma_f32_16x16x32_bf16 v[40:43], v[80:83], v[108:111], v[40:43]\n"
    "v_mfma_f32_16x16x32_bf16 v[40:43], v[88:91], v[96:99], v[40:43]\n"
    "v_add_f32_dpp v32, v33, v32 wave_shr:1 row_mask:0xf bank_mask:0xf\n"
    "v_fmac_f32_dpp v38, v35, v243 wave_ror:1 row_mask:0xf bank_mask:0xf\n"
    "s_waitcnt lgkmcnt(11)\n"
    "v_mfma_f32_16x16x32_bf16 v[40:43], v[112:115], v[84:87], v[40:43]\n"
    "v_add_f32_dpp v38, v39, v38 wave_shr:1 row_mask:0xf bank_mask:0xf\n"
    "v_fmac_f32_dpp v37, v34, v243 wave_ror:1 row_mask:0xf bank_mask:0xf\n"
    "v_mfma_f32_16x16x32_bf16 v[44:47], v[140:143], v[200:203], v[44:47]\n"
    "v_fmac_f32_dpp v36, v33, v243 wave_ror:1 row_mask:0xf bank_mask:0xf\n"
    "v_add_f32_dpp v37, v38, v37 wave_shr:1 row_mask:0xf bank_mask:0xf\n"
    "v_mfma_f32_16x16x32_bf16 v[44:47], v[92:95], v[212:215], v[44:47]\n"
    "v_mfma_f32_16x16x32_bf16 v[44:47], v[100:103], v[120:123], v[44:47]\n"
    "v_add_f32_dpp v36, v37, v36 wave_shr:1 row_mask:0xf bank_mask:0xf\n"
    "v_fmac_f32_dpp v42, v39, v243 wave_ror:1 row_mask:0xf bank_mask:0xf\n"
    "v_mfma_f32_16x16x32_bf16 v[44:47], v[136:139], v[108:111], v[44:47]\n"
    "v_add_f32_dpp v42, v43, v42 wave_shr:1 row_mask:0xf bank_mask:0xf\n"
    "v_fmac_f32_dpp v41, v38, v243 wave_ror:1 row_mask:0xf bank_mask:0xf\n"
    "v_mfma_f32_16x16x32_bf16 v[44:47], v[104:107], v[96:99], v[44:47]\n"
    "v_fmac_f32_dpp v40, v37, v243 wave_ror:1 row_mask:0xf bank_mask:0xf\n"
    "v_add_f32_dpp v41, v42, v41 wave_shr:1 row_mask:0xf bank_mask:0xf\n"
    "s_waitcnt lgkmcnt(9)\n"
    "v_mfma_f32_16x16x32_bf16 v[44:47], v[132:135], v[84:87], v[44:47]\n"
    "v_add_f32_dpp v40, v41, v40 wave_shr:1 row_mask:0xf bank_mask:0xf\n"
    "v_mfma_f32_16x16x32_bf16 v[48:51], v[208:211], v[188:191], v[48:51]\n"
    "v_mfma_f32_16x16x32_bf16 v[48:51], v[216:219], v[200:203], v[48:51]\n"
    "v_mfma_f32_16x16x32_bf16 v[48:51], v[220:223], v[212:215], v[48:51]\n"
    "v_mfma_f32_16x16x32_bf16 v[48:51], v[80:83], v[120:123], v[48:51]\n"
    "v_mfma_f32_16x16x32_bf16 v[48:51], v[88:91], v[108:111], v[48:51]\n"
    "v_mfma_f32_16x16x32_bf16 v[48:51], v[112:115], v[96:99], v[48:51]\n"
    "s_waitcnt lgkmcnt(7)\n"
    "v_mfma_f32_16x16x32_bf16 v[48:51], v[116:119], v[84:87], v[48:51]\n"
    "v_fmac_f32_dpp v46, v43, v243 wave_ror:1 row_mask:0xf bank_mask:0xf\n"
    "v_add_f32_dpp v46, v47, v46 wave_shr:1 row_mask:0xf bank_mask:0xf\n"
    "v_mfma_f32_16x16x32_bf16 v[52:55], v[140:143], v[188:191], v[52:55]\n"
    "v_fmac_f32_dpp v45, v42, v243 wave_ror:1 row_mask:0xf bank_mask:0xf\n"
    "v_fmac_f32_dpp v44, v41, v243 wave_ror:1 row_mask:0xf bank_mask:0xf\n"
    "v_mfma_f32_16x16x32_bf16 v[52:55], v[92:95], v[200:203], v[52:55]\n"
    "v_add_f32_dpp v45, v46, v45 wave_shr:1 row_mask:0xf bank_mask:0xf\n"
    "v_mfma_f32_16x16x32_bf16 v[52:55], v[100:103], v[212:215], v[52:55]\n"
    "v_mfma_f32_16x16x32_bf16 v[52:55], v[136:139], v[120:123], v[52:55]\n"
    "ds_read_b32 v228, v254\n"
    "v_add_f32_dpp v44, v45, v44 wave_shr:1 row_mask:0xf bank_mask:0xf\n"
    "v_mfma_f32_16x16x32_bf16 v[52:55], v[104:107], v[108:111], v[52:55]\n"
    "v_cvt_pk_bf16_f32 v69, v40, v44\n"
    "v_fmac_f32_dpp v50, v47, v243 wave_ror:1 row_mask:0xf bank_mask:0xf\n"
    "v_mfma_f32_16x16x32_bf16 v[52:55], v[132:135], v[96:99], v[52:55]\n"
    "v_add_f32_dpp v50, v51, v50 wave_shr:1 row_mask:0xf bank_mask:0xf\n"
    "v_fmac_f32_dpp v49, v46, v243 wave_ror:1 row_mask:0xf bank_mask:0xf\n"
    "s_waitcnt lgkmcnt(6)\n"
    "v_mfma_f32_16x16x32_bf16 v[52:55], v[124:127], v[84:87], v[52:55]\n"
    "v_fmac_f32_dpp v48, v45, v243 wave_ror:1 row_mask:0xf bank_mask:0xf\n"
    "v_add_f32_dpp v49, v50, v49 wave_shr:1 row_mask:0xf bank_mask:0xf\n"
    "v_mfma_f32_16x16x32_bf16 v[56:59], v[208:211], v[172:175], v[56:59]\n"
    "v_mfma_f32_16x16x32_bf16 v[56:59], v[216:219], v[188:191], v[56:59]\n"
    "s_waitcnt lgkmcnt(0)\n"
    "v_cmp_eq_u32 vcc, -1, v228\n"
    "s_cbranch_vccz .Lcfd29_%=\n"
    "s_mov_b32 s63, 0x4000000\n"
    ".Lcf28_%=:\n"
    "ds_read_b32 v228, v254\n"
    "s_waitcnt lgkmcnt(0)\n"
    "v_cmp_eq_u32 vcc, -1, v228\n"
    "s_cbranch_vccz .Lcfd29_%=\n"
    "s_sub_u32 s63, s63, 1\n"
    "s_cbranch_scc0 .Lcf28_%=\n"
    ".Lcfd29_%=:\n"
    "v_add_f32 v32, v32, v228\n"
    "v_cvt_pk_bf16_f32 v68, v32, v36\n"
    "v_mfma_f32_16x16x32_bf16 v[56:59], v[220:223], v[200:203], v[56:59]\n"
    "v_add_f32_dpp v48, v49, v48 wave_shr:1 row_mask:0xf bank_mask:0xf\n"
    "v_fmac_f32_dpp v54, v51, v243 wave_ror:1 row_mask:0xf bank_mask:0xf\n"
    "v_mfma_f32_16x16x32_bf16 v[56:59], v[80:83], v[212:215], v[56:59]\n"
    "v_add_f32_dpp v54, v55, v54 wave_shr:1 row_mask:0xf bank_mask:0xf\n"
    "v_fmac_f32_dpp v53, v50, v243 wave_ror:1 row_mask:0xf bank_mask:0xf\n"
    "v_mfma_f32_16x16x32_bf16 v[56:59], v[88:91], v[120:123], v[56:59]\n"
    "v_fmac_f32_dpp v52, v49, v243 wave_ror:1 row_mask:0xf bank_mask:0xf\n"
    "v_add_f32_dpp v53, v54, v53 wave_shr:1 row_mask:0xf bank_mask:0xf\n"
    "v_mfma_f32_16x16x32_bf16 v[56:59], v[112:115], v[108:111], v[56:59]\n"
    "v_mfma_f32_16x16x32_bf16 v[56:59], v[116:119], v[96:99], v[56:59]\n"
    "ds_write_b128 v251, v[64:67] offset:0\n"
    "ds_read_b64_tr_b16 v[72:73], v252 offset:0\n"
    "v_mfma_f32_16x16x32_bf16 v[56:59], v[128:131], v[84:87], v[56:59]\n"
    "ds_read_b64_tr_b16 v[74:75], v252 offset:64\n"
    "v_add_f32_dpp v52, v53, v52 wave_shr:1 row_mask:0xf bank_mask:0xf\n"
    "v_mfma_f32_16x16x32_bf16 v[60:63], v[140:143], v[172:175], v[60:63]\n"
    "v_cvt_pk_bf16_f32 v70, v48, v52\n"
    "v_mfma_f32_16x16x32_bf16 v[60:63], v[92:95], v[188:191], v[60:63]\n"
    "v_mfma_f32_16x16x32_bf16 v[60:63], v[100:103], v[200:203], v[60:63]\n"
    "v_mfma_f32_16x16x32_bf16 v[60:63], v[136:139], v[212:215], v[60:63]\n"
    "v_mfma_f32_16x16x32_bf16 v[60:63], v[104:107], v[120:123], v[60:63]\n"
    "v_fmac_f32_dpp v58, v55, v243 wave_ror:1 row_mask:0xf bank_mask:0xf\n"
    "v_add_f32_dpp v58, v59, v58 wave_shr:1 row_mask:0xf bank_mask:0xf\n"
    "v_mfma_f32_16x16x32_bf16 v[60:63], v[132:135], v[108:111], v[60:63]\n"
    "v_fmac_f32_dpp v57, v54, v243 wave_ror:1 row_mask:0xf bank_mask:0xf\n"
    "v_fmac_f32_dpp v56, v53, v243 wave_ror:1 row_mask:0xf bank_mask:0xf\n"
    "v_mfma_f32_16x16x32_bf16 v[60:63], v[124:127], v[96:99], v[60:63]\n"
    "s_waitcnt lgkmcnt(0)\n"
    "buffer_store_dwordx4 v[72:75], v250, s[20:23], 0 offen offset:0 sc1\n"
    "v_add_f32_dpp v57, v58, v57 wave_shr:1 row_mask:0xf bank_mask:0xf\n"
    "v_mfma_f32_16x16x32_bf16 v[60:63], v[156:159], v[84:87], v[60:63]\n"
    "s_nop 0\n"
    "v_add_f32_dpp v56, v57, v56 wave_shr:1 row_mask:0xf bank_mask:0xf\n"
    "s_nop 5\n"
    "v_fmac_f32_dpp v62, v59, v243 wave_ror:1 row_mask:0xf bank_mask:0xf\n"
    "v_add_f32_dpp v62, v63, v62 wave_shr:1 row_mask:0xf bank_mask:0xf\n"
    "v_fmac_f32_dpp v61, v58, v243 wave_ror:1 row_mask:0xf bank_mask:0xf\n"
    "v_fmac_f32_dpp v60, v57, v243 wave_ror:1 row_mask:0xf bank_mask:0xf\n"
    "v_add_f32_dpp v61, v62, v61 wave_shr:1 row_mask:0xf bank_mask:0xf\n"
    "s_nop 1\n"
    "v_add_f32_dpp v60, v61, v60 wave_shr:1 row_mask:0xf bank_mask:0xf\n"
    "v_cvt_pk_bf16_f32 v71, v56, v60\n"
    "ds_write_b128 v251, v[68:71] offset:1024\n"
    "ds_read_b64_tr_b16 v[76:77], v252 offset:1024\n"
    "ds_read_b64_tr_b16 v[78:79], v252 offset:1088\n"
    "s_waitcnt lgkmcnt(0)\n"
    "buffer_store_dwordx4 v[76:79], v250, s[20:23], 0 offen offset:3072 sc1\n"
    "s_or_b32 s58, s2, s48\n"
    "s_cmp_lg_u32 s58, 0\n"
    "s_cbranch_scc1 .LZ27_%=\n"
    "v_lshlrev_b32 v2, 11, v255\n"
    "s_mov_b32 s58, 10616832\n"
    "buffer_load_dwordx2 v[4:5], v2, s[20:23], s58 offen offset:0 sc1\n"
    "buffer_load_dwordx2 v[6:7], v2, s[20:23], s58 offen offset:512 sc1\n"
    "buffer_load_dwordx2 v[8:9], v2, s[20:23], s58 offen offset:1024 sc1\n"
    "buffer_load_dwordx2 v[10:11], v2, s[20:23], s58 offen offset:1536 sc1\n"
    "s_waitcnt vmcnt(3)\n"
    "v_cmp_ne_u64 vcc, s[46:47], v[4:5]\n"
    "s_waitcnt vmcnt(2)\n"
    "v_cmp_ne_u64_e64 s[60:61], s[46:47], v[6:7]\n"
    "s_or_b64 vcc, vcc, s[60:61]\n"
    "s_waitcnt vmcnt(1)\n"
    "v_cmp_ne_u64_e64 s[60:61], s[46:47], v[8:9]\n"
    "s_or_b64 vcc, vcc, s[60:61]\n"
    "s_waitcnt vmcnt(0)\n"
    "v_cmp_ne_u64_e64 s[60:61], s[46:47], v[10:11]\n"
    "s_or_b64 vcc, vcc, s[60:61]\n"
    "s_cbranch_vccz .Lspd31_%=\n"
    "s_mov_b32 s63, 0x1000000\n"
    ".Lsp30_%=:\n"
    "s_sleep 4\n"
    "buffer_load_dwordx2 v[4:5], v2, s[20:23], s58 offen offset:0 sc1\n"
    "buffer_load_dwordx2 v[6:7], v2, s[20:23], s58 offen offset:512 sc1\n"
    "buffer_load_dwordx2 v[8:9], v2, s[20:23], s58 offen offset:1024 sc1\n"
    "buffer_load_dwordx2 v[10:11], v2, s[20:23], s58 offen offset:1536 sc1\n"
    "s_waitcnt vmcnt(0)\n"
    "v_cmp_ne_u64 vcc, s[46:47], v[4:5]\n"
    "v_cmp_ne_u64_e64 s[60:61], s[46:47], v[6:7]\n"
    "s_or_b64 vcc, vcc, s[60:61]\n"
    "v_cmp_ne_u64_e64 s[60:61], s[46:47], v[8:9]\n"
    "s_or_b64 vcc, vcc, s[60:61]\n"
    "v_cmp_ne_u64_e64 s[60:61], s[46:47], v[10:11]\n"
    "s_or_b64 vcc, vcc, s[60:61]\n"
    "s_cbranch_vccz .Lspd31_%=\n"
    "s_sub_u32 s63, s63, 1\n"
    "s_cbranch_scc0 .Lsp30_%=\n"
    ".Lspd31_%=:\n"
    "s_mov_b64 exec, 1\n"
    "buffer_store_dwordx2 v[244:245], off, s[20:23], 0 sc1\n"
    "s_mov_b64 exec, -1\n"
    "s_branch .LZ27_%=\n"
    ".p2align 3\n"
    ".LY26_%=:\n"
    "ds_read_b64 v[80:81], v248 offset:256\n"
    "ds_read_b64 v[82:83], v248 offset:264\n"
    "ds_read_b64_tr_b16 v[84:85], v249 offset:64\n"
    "ds_read_b64_tr_b16 v[86:87], v249 offset:0\n"
    "ds_read_b64 v[88:89], v248 offset:192\n"
    "ds_read_b64 v[90:91], v248 offset:200\n"
    "s_waitcnt lgkmcnt(2)\n"
    "v_mfma_f32_16x16x32_bf16 v[0:3], v[80:83], v[84:87], 0\n"
    "ds_read_b64_tr_b16 v[92:93], v249 offset:576\n"
    "ds_read_b64_tr_b16 v[94:95], v249 offset:512\n"
    "ds_read_b64 v[96:97], v248 offset:128\n"
    "s_waitcnt lgkmcnt(1)\n"
    "v_mfma_f32_16x16x32_bf16 v[0:3], v[88:91], v[92:95], v[0:3]\n"
    "ds_read_b64 v[98:99], v248 offset:136\n"
    "ds_read_b64_tr_b16 v[100:101], v249 offset:1088\n"
    "ds_read_b64_tr_b16 v[102:103], v249 offset:1024\n"
    "s_waitcnt lgkmcnt(0)\n"
    "v_mfma_f32_16x16x32_bf16 v[0:3], v[96:99], v[100:103], v[0:3]\n"
    "ds_read_b64 v[104:105], v248 offset:64\n"
    "ds_read_b64 v[106:107], v248 offset:72\n"
    "ds_read_b64_tr_b16 v[108:109], v249 offset:1600\n"
    "ds_read_b64_tr_b16 v[110:111], v249 offset:1536\n"
    "s_waitcnt lgkmcnt(0)\n"
    "v_mfma_f32_16x16x32_bf16 v[0:3], v[104:107], v[108:111], v[0:3]\n"
    "ds_read_b64 v[112:113], v248 offset:0\n"
    "ds_read_b64 v[114:115], v248 offset:8\n"
    "ds_read_b64_tr_b16 v[116:117], v249 offset:2112\n"
    "ds_read_b64_tr_b16 v[118:119], v249 offset:2048\n"
    "s_waitcnt lgkmcnt(0)\n"
    "v_mfma_f32_16x16x32_bf16 v[0:3], v[112:115], v[116:119], v[0:3]\n"
    "ds_read_b64 v[120:121], v248 offset:288\n"
    "ds_read_b64 v[122:123], v248 offset:296\n"
    "ds_read_b64 v[124:125], v248 offset:224\n"
    "s_waitcnt lgkmcnt(1)\n"
    "v_mfma_f32_16x16x32_bf16 v[4:7], v[120:123], v[84:87], 0\n"
    "ds_read_b64 v[126:127], v248 offset:232\n"
    "ds_read_b64 v[128:129], v248 offset:160\n"
    "ds_read_b64 v[130:131], v248 offset:168\n"
    "s_waitcnt lgkmcnt(2)\n"
    "v_mfma_f32_16x16x32_bf16 v[4:7], v[124:127], v[92:95], v[4:7]\n"
    "ds_read_b64 v[132:133], v248 offset:96\n"
    "ds_read_b64 v[134:135], v248 offset:104\n"
    "ds_read_b64 v[136:137], v248 offset:32\n"
    "s_waitcnt lgkmcnt(3)\n"
    "v_mfma_f32_16x16x32_bf16 v[4:7], v[128:131], v[100:103], v[4:7]\n"
    "ds_read_b64 v[138:139], v248 offset:40\n"
    "ds_read_b64 v[140:141], v248 offset:320\n"
    "ds_read_b64 v[142:143], v248 offset:328\n"
    "s_waitcnt lgkmcnt(4)\n"
    "v_mfma_f32_16x16x32_bf16 v[4:7], v[132:135], v[108:111], v[4:7]\n"
    "ds_read_b64_tr_b16 v[144:145], v249 offset:2624\n"
    "ds_read_b64_tr_b16 v[146:147], v249 offset:2560\n"
    "s_waitcnt lgkmcnt(4)\n"
    "v_mfma_f32_16x16x32_bf16 v[4:7], v[136:139], v[116:119], v[4:7]\n"
    "ds_read_b64 v[148:149], v248 offset:352\n"
    "ds_read_b64 v[150:151], v248 offset:360\n"
    "s_waitcnt lgkmcnt(4)\n"
    "s_nop 0\n"
    "v_mfma_f32_16x16x32_bf16 v[8:11], v[140:143], v[84:87], 0\n"
    "v_add_f32_dpp v2, v3, v2 wave_shr:1 row_mask:0xf bank_mask:0xf\n"
    "ds_read_b64 v[152:153], v248 offset:384\n"
    "v_mfma_f32_16x16x32_bf16 v[8:11], v[80:83], v[92:95], v[8:11]\n"
    "v_add_f32_dpp v1, v2, v1 wave_shr:1 row_mask:0xf bank_mask:0xf\n"
    "ds_read_b64 v[154:155], v248 offset:392\n"
    "v_mfma_f32_16x16x32_bf16 v[8:11], v[88:91], v[100:103], v[8:11]\n"
    "v_add_f32_dpp v0, v1, v0 wave_shr:1 row_mask:0xf bank_mask:0xf\n"
    "v_fmac_f32_dpp v6, v3, v243 wave_ror:1 row_mask:0xf bank_mask:0xf\n"
    "v_mfma_f32_16x16x32_bf16 v[8:11], v[96:99], v[108:111], v[8:11]\n"
    "v_add_f32_dpp v6, v7, v6 wave_shr:1 row_mask:0xf bank_mask:0xf\n"
    "v_fmac_f32_dpp v5, v2, v243 wave_ror:1 row_mask:0xf bank_mask:0xf\n"
    "v_mfma_f32_16x16x32_bf16 v[8:11], v[104:107], v[116:119], v[8:11]\n"
    "v_fmac_f32_dpp v4, v1, v243 wave_ror:1 row_mask:0xf bank_mask:0xf\n"
    "v_add_f32_dpp v5, v6, v5 wave_shr:1 row_mask:0xf bank_mask:0xf\n"
    "s_waitcnt lgkmcnt(4)\n"
    "v_mfma_f32_16x16x32_bf16 v[8:11], v[112:115], v[144:147], v[8:11]\n"
    "v_add_f32_dpp v4, v5, v4 wave_shr:1 row_mask:0xf bank_mask:0xf\n"
    "ds_read_b64_tr_b16 v[156:157], v249 offset:3136\n"
    "s_waitcnt lgkmcnt(3)\n"
    "v_mfma_f32_16x16x32_bf16 v[12:15], v[148:151], v[84:87], 0\n"
    "ds_read_b64_tr_b16 v[158:159], v249 offset:3072\n"
    "ds_read_b64 v[160:161], v248 offset:416\n"
    "v_mfma_f32_16x16x32_bf16 v[12:15], v[120:123], v[92:95], v[12:15]\n"
    "ds_read_b64 v[162:163], v248 offset:424\n"
    "ds_read_b64 v[164:165], v248 offset:448\n"
    "v_mfma_f32_16x16x32_bf16 v[12:15], v[124:127], v[100:103], v[12:15]\n"
    "v_fmac_f32_dpp v10, v7, v243 wave_ror:1 row_mask:0xf bank_mask:0xf\n"
    "v_add_f32_dpp v10, v11, v10 wave_shr:1 row_mask:0xf bank_mask:0xf\n"
    "v_mfma_f32_16x16x32_bf16 v[12:15], v[128:131], v[108:111], v[12:15]\n"
    "v_fmac_f32_dpp v9, v6, v243 wave_ror:1 row_mask:0xf bank_mask:0xf\n"
    "v_fmac_f32_dpp v8, v5, v243 wave_ror:1 row_mask:0xf bank_mask:0xf\n"
    "v_mfma_f32_16x16x32_bf16 v[12:15], v[132:135], v[116:119], v[12:15]\n"
    "v_add_f32_dpp v9, v10, v9 wave_shr:1 row_mask:0xf bank_mask:0xf\n"
    "ds_read_b64 v[166:167], v248 offset:456\n"
    "v_mfma_f32_16x16x32_bf16 v[12:15], v[136:139], v[144:147], v[12:15]\n"
    "v_add_f32_dpp v8, v9, v8 wave_shr:1 row_mask:0xf bank_mask:0xf\n"
    "ds_read_b64_tr_b16 v[168:169], v249 offset:3648\n"
    "s_waitcnt lgkmcnt(7)\n"
    "s_nop 0\n"
    "v_mfma_f32_16x16x32_bf16 v[16:19], v[152:155], v[84:87], 0\n"
    "ds_read_b64_tr_b16 v[170:171], v249 offset:3584\n"
    "ds_read_b64 v[172:173], v248 offset:480\n"
    "v_mfma_f32_16x16x32_bf16 v[16:19], v[140:143], v[92:95], v[16:19]\n"
    "ds_read_b64 v[174:175], v248 offset:488\n"
    "ds_read_b64 v[180:181], v248 offset:736\n"
    "v_mfma_f32_16x16x32_bf16 v[16:19], v[80:83], v[100:103], v[16:19]\n"
    "v_fmac_f32_dpp v14, v11, v243 wave_ror:1 row_mask:0xf bank_mask:0xf\n"
    "v_add_f32_dpp v14, v15, v14 wave_shr:1 row_mask:0xf bank_mask:0xf\n"
    "v_mfma_f32_16x16x32_bf16 v[16:19], v[88:91], v[108:111], v[16:19]\n"
    "v_fmac_f32_dpp v13, v10, v243 wave_ror:1 row_mask:0xf bank_mask:0xf\n"
    "v_fmac_f32_dpp v12, v9, v243 wave_ror:1 row_mask:0xf bank_mask:0xf\n"
    "v_mfma_f32_16x16x32_bf16 v[16:19], v[96:99], v[116:119], v[16:19]\n"
    "v_add_f32_dpp v13, v14, v13 wave_shr:1 row_mask:0xf bank_mask:0xf\n"
    "ds_read_b64 v[182:183], v248 offset:744\n"
    "v_mfma_f32_16x16x32_bf16 v[16:19], v[104:107], v[144:147], v[16:19]\n"
    "v_add_f32_dpp v12, v13, v12 wave_shr:1 row_mask:0xf bank_mask:0xf\n"
    "v_cvt_pk_bf16_f32 v65, v8, v12\n"
    "s_waitcnt lgkmcnt(10)\n"
    "v_mfma_f32_16x16x32_bf16 v[16:19], v[112:115], v[156:159], v[16:19]\n"
    "ds_read_b64 v[184:185], v248 offset:672\n"
    "ds_read_b64 v[186:187], v248 offset:680\n"
    "s_waitcnt lgkmcnt(10)\n"
    "v_mfma_f32_16x16x32_bf16 v[20:23], v[160:163], v[84:87], 0\n"
    "ds_read_b64 v[188:189], v248 offset:608\n"
    "ds_read_b64 v[190:191], v248 offset:616\n"
    "v_mfma_f32_16x16x32_bf16 v[20:23], v[148:151], v[92:95], v[20:23]\n"
    "ds_read_b64 v[192:193], v248 offset:544\n"
    "ds_read_b64 v[194:195], v248 offset:552\n"
    "v_mfma_f32_16x16x32_bf16 v[20:23], v[120:123], v[100:103], v[20:23]\n"
    "v_fmac_f32_dpp v18, v15, v243 wave_ror:1 row_mask:0xf bank_mask:0xf\n"
    "v_add_f32_dpp v18, v19, v18 wave_shr:1 row_mask:0xf bank_mask:0xf\n"
    "v_mfma_f32_16x16x32_bf16 v[20:23], v[124:127], v[108:111], v[20:23]\n"
    "v_fmac_f32_dpp v17, v14, v243 wave_ror:1 row_mask:0xf bank_mask:0xf\n"
    "v_fmac_f32_dpp v16, v13, v243 wave_ror:1 row_mask:0xf bank_mask:0xf\n"
    "v_mfma_f32_16x16x32_bf16 v[20:23], v[128:131], v[116:119], v[20:23]\n"
    "v_add_f32_dpp v17, v18, v17 wave_shr:1 row_mask:0xf bank_mask:0xf\n"
    "ds_read_b64_tr_b16 v[196:197], v249 offset:4160\n"
    "v_mfma_f32_16x16x32_bf16 v[20:23], v[132:135], v[144:147], v[20:23]\n"
    "v_add_f32_dpp v16, v17, v16 wave_shr:1 row_mask:0xf bank_mask:0xf\n"
    "ds_read_b64_tr_b16 v[198:199], v249 offset:4096\n"
    "v_mfma_f32_16x16x32_bf16 v[20:23], v[136:139], v[156:159], v[20:23]\n"
    "ds_read_b64_tr_b16 v[200:201], v249 offset:4672\n"
    "ds_read_b64_tr_b16 v[202:203], v249 offset:4608\n"
    "s_waitcnt lgkmcnt(15)\n"
    "s_nop 0\n"
    "v_mfma_f32_16x16x32_bf16 v[24:27], v[164:167], v[84:87], 0\n"
    "ds_read_b64_tr_b16 v[204:205], v249 offset:5184\n"
    "ds_read_b64_tr_b16 v[206:207], v249 offset:5120\n"
    "v_mfma_f32_16x16x32_bf16 v[24:27], v[152:155], v[92:95], v[24:27]\n"
    "ds_read_b64_tr_b16 v[208:209], v249 offset:5696\n"
    "ds_read_b64_tr_b16 v[210:211], v249 offset:5632\n"
    "v_mfma_f32_16x16x32_bf16 v[24:27], v[140:143], v[100:103], v[24:27]\n"
    "v_fmac_f32_dpp v22, v19, v243 wave_ror:1 row_mask:0xf bank_mask:0xf\n"
    "v_add_f32_dpp v22, v23, v22 wave_shr:1 row_mask:0xf bank_mask:0xf\n"
    "v_mfma_f32_16x16x32_bf16 v[24:27], v[80:83], v[108:111], v[24:27]\n"
    "v_fmac_f32_dpp v21, v18, v243 wave_ror:1 row_mask:0xf bank_mask:0xf\n"
    "v_fmac_f32_dpp v20, v17, v243 wave_ror:1 row_mask:0xf bank_mask:0xf\n"
    "v_mfma_f32_16x16x32_bf16 v[24:27], v[88:91], v[116:119], v[24:27]\n"
    "v_add_f32_dpp v21, v22, v21 wave_shr:1 row_mask:0xf bank_mask:0xf\n"
    "ds_read_b64 v[212:213], v248 offset:512\n"
    "v_mfma_f32_16x16x32_bf16 v[24:27], v[96:99], v[144:147], v[24:27]\n"
    "v_add_f32_dpp v20, v21, v20 wave_shr:1 row_mask:0xf bank_mask:0xf\n"
    "v_cvt_pk_bf16_f32 v66, v16, v20\n"
    "v_mfma_f32_16x16x32_bf16 v[24:27], v[104:107], v[156:159], v[24:27]\n"
    "ds_read_b64 v[214:215], v248 offset:520\n"
    "ds_read_b64 v[216:217], v248 offset:576\n"
    "s_waitcnt lgkmcnt(15)\n"
    "v_mfma_f32_16x16x32_bf16 v[24:27], v[112:115], v[168:171], v[24:27]\n"
    "ds_read_b64 v[218:219], v248 offset:584\n"
    "ds_read_b64 v[220:221], v248 offset:640\n"
    "v_mfma_f32_16x16x32_bf16 v[28:31], v[172:175], v[84:87], 0\n"
    "ds_read_b64 v[222:223], v248 offset:648\n"
    "v_mfma_f32_16x16x32_bf16 v[28:31], v[160:163], v[92:95], v[28:31]\n"
    "s_waitcnt vmcnt(0)\n"
    "s_mov_b64 exec, 1\n"
    "buffer_store_dwordx2 v[244:245], off, s[20:23], s72 sc1\n"
    "s_mov_b64 exec, -1\n"
    "v_mfma_f32_16x16x32_bf16 v[28:31], v[148:151], v[100:103], v[28:31]\n"
    "v_fmac_f32_dpp v26, v23, v243 wave_ror:1 row_mask:0xf bank_mask:0xf\n"
    "v_add_f32_dpp v26, v27, v26 wave_shr:1 row_mask:0xf bank_mask:0xf\n"
    "v_mfma_f32_16x16x32_bf16 v[28:31], v[120:123], v[108:111], v[28:31]\n"
    "v_fmac_f32_dpp v25, v22, v243 wave_ror:1 row_mask:0xf bank_mask:0xf\n"
    "v_fmac_f32_dpp v24, v21, v243 wave_ror:1 row_mask:0xf bank_mask:0xf\n"
    "v_mfma_f32_16x16x32_bf16 v[28:31], v[124:127], v[116:119], v[28:31]\n"
    "v_add_f32_dpp v25, v26, v25 wave_shr:1 row_mask:0xf bank_mask:0xf\n"
    "v_mfma_f32_16x16x32_bf16 v[28:31], v[128:131], v[144:147], v[28:31]\n"
    "v_mfma_f32_16x16x32_bf16 v[28:31], v[132:135], v[156:159], v[28:31]\n"
    "v_add_f32_dpp v24, v25, v24 wave_shr:1 row_mask:0xf bank_mask:0xf\n"
    "v_mfma_f32_16x16x32_bf16 v[28:31], v[136:139], v[168:171], v[28:31]\n"
    "v_mfma_f32_16x16x32_bf16 v[60:63], v[180:183], v[84:87], 0\n"
    "ds_read_b64 v[180:181], v248 offset:704\n"
    "ds_read_b64 v[182:183], v248 offset:712\n"
    "v_mfma_f32_16x16x32_bf16 v[60:63], v[184:187], v[92:95], v[60:63]\n"
    "s_waitcnt lgkmcnt(15)\n"
    "v_mfma_f32_16x16x32_bf16 v[60:63], v[188:191], v[100:103], v[60:63]\n"
    "v_mfma_f32_16x16x32_bf16 v[60:63], v[192:195], v[108:111], v[60:63]\n"
    "v_mfma_f32_16x16x32_bf16 v[60:63], v[172:175], v[116:119], v[60:63]\n"
    "v_fmac_f32_dpp v30, v27, v243 wave_ror:1 row_mask:0xf bank_mask:0xf\n"
    "v_add_f32_dpp v30, v31, v30 wave_shr:1 row_mask:0xf bank_mask:0xf\n"
    "v_mfma_f32_16x16x32_bf16 v[60:63], v[160:163], v[144:147], v[60:63]\n"
    "v_fmac_f32_dpp v29, v26, v243 wave_ror:1 row_mask:0xf bank_mask:0xf\n"
    "v_fmac_f32_dpp v28, v25, v243 wave_ror:1 row_mask:0xf bank_mask:0xf\n"
    "v_mfma_f32_16x16x32_bf16 v[60:63], v[148:151], v[156:159], v[60:63]\n"
    "v_add_f32_dpp v29, v30, v29 wave_shr:1 row_mask:0xf bank_mask:0xf\n"
    "v_mfma_f32_16x16x32_bf16 v[60:63], v[120:123], v[168:171], v[60:63]\n"
    "s_waitcnt lgkmcnt(14)\n"
    "v_mfma_f32_16x16x32_bf16 v[60:63], v[124:127], v[196:199], v[60:63]\n"
    "v_add_f32_dpp v28, v29, v28 wave_shr:1 row_mask:0xf bank_mask:0xf\n"
    "v_cvt_pk_bf16_f32 v67, v24, v28\n"
    "s_waitcnt lgkmcnt(12)\n"
    "v_mfma_f32_16x16x32_bf16 v[60:63], v[128:131], v[200:203], v[60:63]\n"
    "s_waitcnt lgkmcnt(10)\n"
    "v_mfma_f32_16x16x32_bf16 v[60:63], v[132:135], v[204:207], v[60:63]\n"
    "s_waitcnt lgkmcnt(8)\n"
    "v_mfma_f32_16x16x32_bf16 v[60:63], v[136:139], v[208:211], v[60:63]\n"
    "s_waitcnt lgkmcnt(6)\n"
    "v_mfma_f32_16x16x32_bf16 v[32:35], v[212:215], v[84:87], 0\n"
    "v_mfma_f32_16x16x32_bf16 v[32:35], v[164:167], v[92:95], v[32:35]\n"
    "v_mfma_f32_16x16x32_bf16 v[32:35], v[152:155], v[100:103], v[32:35]\n"
    "v_mfma_f32_16x16x32_bf16 v[32:35], v[140:143], v[108:111], v[32:35]\n"
    "v_mfma_f32_16x16x32_bf16 v[32:35], v[80:83], v[116:119], v[32:35]\n"
    "v_mfma_f32_16x16x32_bf16 v[32:35], v[88:91], v[144:147], v[32:35]\n"
    "v_mfma_f32_16x16x32_bf16 v[32:35], v[96:99], v[156:159], v[32:35]\n"
    "v_add_f32_dpp v226, v63, v62 wave_shr:1 row_mask:0xf bank_mask:0xf\n"
    "v_mov_b32_e64 v227, v63\n"
    "v_mfma_f32_16x16x32_bf16 v[32:35], v[104:107], v[168:171], v[32:35]\n"
    "v_add_f32_dpp v225, v226, v61 wave_shr:1 row_mask:0xf bank_mask:0xf\n"
    "v_mfma_f32_16x16x32_bf16 v[32:35], v[112:115], v[196:199], v[32:35]\n"
    "v_mfma_f32_16x16x32_bf16 v[36:39], v[192:195], v[84:87], 0\n"
    "v_mfma_f32_16x16x32_bf16 v[36:39], v[172:175], v[92:95], v[36:39]\n"
    "v_mfma_f32_16x16x32_bf16 v[36:39], v[160:163], v[100:103], v[36:39]\n"
    "v_mfma_f32_16x16x32_bf16 v[36:39], v[148:151], v[108:111], v[36:39]\n"
    "v_mfma_f32_16x16x32_bf16 v[36:39], v[120:123], v[116:119], v[36:39]\n"
    "v_mfma_f32_16x16x32_bf16 v[36:39], v[124:127], v[144:147], v[36:39]\n"
    "v_mfma_f32_16x16x32_bf16 v[36:39], v[128:131], v[156:159], v[36:39]\n"
    "v_mfma_f32_16x16x32_bf16 v[36:39], v[132:135], v[168:171], v[36:39]\n"
    "v_fmac_f32_dpp v34, v31, v243 wave_ror:1 row_mask:0xf bank_mask:0xf\n"
    "v_add_f32_dpp v34, v35, v34 wave_shr:1 row_mask:0xf bank_mask:0xf\n"
    "v_mfma_f32_16x16x32_bf16 v[36:39], v[136:139], v[196:199], v[36:39]\n"
    "v_fmac_f32_dpp v33, v30, v243 wave_ror:1 row_mask:0xf bank_mask:0xf\n"
    "v_fmac_f32_dpp v32, v29, v243 wave_ror:1 row_mask:0xf bank_mask:0xf\n"
    "s_waitcnt lgkmcnt(4)\n"
    "s_nop 0\n"
    "v_mfma_f32_16x16x32_bf16 v[40:43], v[216:219], v[84:87], 0\n"
    "v_add_f32_dpp v33, v34, v33 wave_shr:1 row_mask:0xf bank_mask:0xf\n"
    "v_mfma_f32_16x16x32_bf16 v[40:43], v[212:215], v[92:95], v[40:43]\n"
    "v_mfma_f32_16x16x32_bf16 v[40:43], v[164:167], v[100:103], v[40:43]\n"
    "v_add_f32_dpp v32, v33, v32 wave_shr:1 row_mask:0xf bank_mask:0xf\n"
    "v_fmac_f32_dpp v38, v35, v243 wave_ror:1 row_mask:0xf bank_mask:0xf\n"
    "v_mfma_f32_16x16x32_bf16 v[40:43], v[152:155], v[108:111], v[40:43]\n"
    "v_add_f32_dpp v38, v39, v38 wave_shr:1 row_mask:0xf bank_mask:0xf\n"
    "v_fmac_f32_dpp v37, v34, v243 wave_ror:1 row_mask:0xf bank_mask:0xf\n"
    "v_mfma_f32_16x16x32_bf16 v[40:43], v[140:143], v[116:119], v[40:43]\n"
    "v_fmac_f32_dpp v36, v33, v243 wave_ror:1 row_mask:0xf bank_mask:0xf\n"
    "v_add_f32_dpp v37, v38, v37 wave_shr:1 row_mask:0xf bank_mask:0xf\n"
    "v_mfma_f32_16x16x32_bf16 v[40:43], v[80:83], v[144:147], v[40:43]\n"
    "v_mfma_f32_16x16x32_bf16 v[40:43], v[88:91], v[156:159], v[40:43]\n"
    "v_add_f32_dpp v36, v37, v36 wave_shr:1 row_mask:0xf bank_mask:0xf\n"
    "v_cvt_pk_bf16_f32 v68, v32, v36\n"
    "v_mfma_f32_16x16x32_bf16 v[40:43], v[96:99], v[168:171], v[40:43]\n"
    "v_mfma_f32_16x16x32_bf16 v[40:43], v[104:107], v[196:199], v[40:43]\n"
    "v_mfma_f32_16x16x32_bf16 v[40:43], v[112:115], v[200:203], v[40:43]\n"
    "v_mfma_f32_16x16x32_bf16 v[44:47], v[188:191], v[84:87], 0\n"
    "v_mfma_f32_16x16x32_bf16 v[44:47], v[192:195], v[92:95], v[44:47]\n"
    "v_mfma_f32_16x16x32_bf16 v[44:47], v[172:175], v[100:103], v[44:47]\n"
    "v_mfma_f32_16x16x32_bf16 v[44:47], v[160:163], v[108:111], v[44:47]\n"
    "v_mfma_f32_16x16x32_bf16 v[44:47], v[148:151], v[116:119], v[44:47]\n"
    "s_mov_b64 exec, s[76:77]\n"
    "ds_write_b128 v253, v[224:227]\n"
    "s_mov_b64 exec, -1\n"
    "v_fmac_f32_dpp v42, v39, v243 wave_ror:1 row_mask:0xf bank_mask:0xf\n"
    "v_mfma_f32_16x16x32_bf16 v[44:47], v[120:123], v[144:147], v[44:47]\n"
    "v_add_f32_dpp v42, v43, v42 wave_shr:1 row_mask:0xf bank_mask:0xf\n"
    "v_fmac_f32_dpp v41, v38, v243 wave_ror:1 row_mask:0xf bank_mask:0xf\n"
    "v_mfma_f32_16x16x32_bf16 v[44:47], v[124:127], v[156:159], v[44:47]\n"
    "v_fmac_f32_dpp v40, v37, v243 wave_ror:1 row_mask:0xf bank_mask:0xf\n"
    "v_add_f32_dpp v41, v42, v41 wave_shr:1 row_mask:0xf bank_mask:0xf\n"
    "v_mfma_f32_16x16x32_bf16 v[44:47], v[128:131], v[168:171], v[44:47]\n"
    "v_mfma_f32_16x16x32_bf16 v[44:47], v[132:135], v[196:199], v[44:47]\n"
    "v_add_f32_dpp v40, v41, v40 wave_shr:1 row_mask:0xf bank_mask:0xf\n"
    "v_mfma_f32_16x16x32_bf16 v[44:47], v[136:139], v[200:203], v[44:47]\n"
    "s_waitcnt lgkmcnt(3)\n"
    "s_nop 0\n"
    "v_mfma_f32_16x16x32_bf16 v[48:51], v[220:223], v[84:87], 0\n"
    "v_mfma_f32_16x16x32_bf16 v[48:51], v[216:219], v[92:95], v[48:51]\n"
    "v_mfma_f32_16x16x32_bf16 v[48:51], v[212:215], v[100:103], v[48:51]\n"
    "v_mfma_f32_16x16x32_bf16 v[48:51], v[164:167], v[108:111], v[48:51]\n"
    "v_mfma_f32_16x16x32_bf16 v[48:51], v[152:155], v[116:119], v[48:51]\n"
    "v_mfma_f32_16x16x32_bf16 v[48:51], v[140:143], v[144:147], v[48:51]\n"
    "v_mfma_f32_16x16x32_bf16 v[48:51], v[80:83], v[156:159], v[48:51]\n"
    "v_fmac_f32_dpp v46, v43, v243 wave_ror:1 row_mask:0xf bank_mask:0xf\n"
    "v_add_f32_dpp v46, v47, v46 wave_shr:1 row_mask:0xf bank_mask:0xf\n"
    "v_mfma_f32_16x16x32_bf16 v[48:51], v[88:91], v[168:171], v[48:51]\n"
    "v_fmac_f32_dpp v45, v42, v243 wave_ror:1 row_mask:0xf bank_mask:0xf\n"
    "v_fmac_f32_dpp v44, v41, v243 wave_ror:1 row_mask:0xf bank_mask:0xf\n"
    "v_mfma_f32_16x16x32_bf16 v[48:51], v[96:99], v[196:199], v[48:51]\n"
    "v_add_f32_dpp v45, v46, v45 wave_shr:1 row_mask:0xf bank_mask:0xf\n"
    "v_mfma_f32_16x16x32_bf16 v[48:51], v[104:107], v[200:203], v[48:51]\n"
    "v_mfma_f32_16x16x32_bf16 v[48:51], v[112:115], v[204:207], v[48:51]\n"
    "v_add_f32_dpp v44, v45, v44 wave_shr:1 row_mask:0xf bank_mask:0xf\n"
    "v_cvt_pk_bf16_f32 v69, v40, v44\n"
    "v_mfma_f32_16x16x32_bf16 v[52:55], v[184:187], v[84:87], 0\n"
    "v_mfma_f32_16x16x32_bf16 v[52:55], v[188:191], v[92:95], v[52:55]\n"
    "v_mfma_f32_16x16x32_bf16 v[52:55], v[192:195], v[100:103], v[52:55]\n"
    "v_mfma_f32_16x16x32_bf16 v[52:55], v[172:175], v[108:111], v[52:55]\n"
    "ds_read_b32 v228, v254\n"
    "v_mfma_f32_16x16x32_bf16 v[52:55], v[160:163], v[116:119], v[52:55]\n"
    "v_fmac_f32_dpp v50, v47, v243 wave_ror:1 row_mask:0xf bank_mask:0xf\n"
    "v_add_f32_dpp v50, v51, v50 wave_shr:1 row_mask:0xf bank_mask:0xf\n"
    "v_mfma_f32_16x16x32_bf16 v[52:55], v[148:151], v[144:147], v[52:55]\n"
    "v_fmac_f32_dpp v49, v46, v243 wave_ror:1 row_mask:0xf bank_mask:0xf\n"
    "v_fmac_f32_dpp v48, v45, v243 wave_ror:1 row_mask:0xf bank_mask:0xf\n"
    "v_mfma_f32_16x16x32_bf16 v[52:55], v[120:123], v[156:159], v[52:55]\n"
    "v_add_f32_dpp v49, v50, v49 wave_shr:1 row_mask:0xf bank_mask:0xf\n"
    "v_mfma_f32_16x16x32_bf16 v[52:55], v[124:127], v[168:171], v[52:55]\n"
    "v_mfma_f32_16x16x32_bf16 v[52:55], v[128:131], v[196:199], v[52:55]\n"
    "s_waitcnt lgkmcnt(0)\n"
    "v_cmp_eq_u32 vcc, -1, v228\n"
    "s_cbranch_vccz .Lcfd33_%=\n"
    "s_mov_b32 s63, 0x4000000\n"
    ".Lcf32_%=:\n"
    "ds_read_b32 v228, v254\n"
    "s_waitcnt lgkmcnt(0)\n"
    "v_cmp_eq_u32 vcc, -1, v228\n"
    "s_cbranch_vccz .Lcfd33_%=\n"
    "s_sub_u32 s63, s63, 1\n"
    "s_cbranch_scc0 .Lcf32_%=\n"
    ".Lcfd33_%=:\n"
    "v_add_f32_e64 v0, v0, v228\n"
    "v_cvt_pk_bf16_f32 v64, v0, v4\n"
    "v_mfma_f32_16x16x32_bf16 v[52:55], v[132:135], v[200:203], v[52:55]\n"
    "v_add_f32_dpp v48, v49, v48 wave_shr:1 row_mask:0xf bank_mask:0xf\n"
    "v_mfma_f32_16x16x32_bf16 v[52:55], v[136:139], v[204:207], v[52:55]\n"
    "v_mfma_f32_16x16x32_bf16 v[56:59], v[180:183], v[84:87], 0\n"
    "v_mfma_f32_16x16x32_bf16 v[56:59], v[220:223], v[92:95], v[56:59]\n"
    "v_mfma_f32_16x16x32_bf16 v[56:59], v[216:219], v[100:103], v[56:59]\n"
    "ds_write_b128 v251, v[64:67] offset:0\n"
    "ds_read_b64_tr_b16 v[72:73], v252 offset:0\n"
    "v_mfma_f32_16x16x32_bf16 v[56:59], v[212:215], v[108:111], v[56:59]\n"
    "ds_read_b64_tr_b16 v[74:75], v252 offset:64\n"
    "v_mfma_f32_16x16x32_bf16 v[56:59], v[164:167], v[116:119], v[56:59]\n"
    "v_fmac_f32_dpp v54, v51, v243 wave_ror:1 row_mask:0xf bank_mask:0xf\n"
    "v_add_f32_dpp v54, v55, v54 wave_shr:1 row_mask:0xf bank_mask:0xf\n"
    "v_mfma_f32_16x16x32_bf16 v[56:59], v[152:155], v[144:147], v[56:59]\n"
    "v_fmac_f32_dpp v53, v50, v243 wave_ror:1 row_mask:0xf bank_mask:0xf\n"
    "v_fmac_f32_dpp v52, v49, v243 wave_ror:1 row_mask:0xf bank_mask:0xf\n"
    "v_mfma_f32_16x16x32_bf16 v[56:59], v[140:143], v[156:159], v[56:59]\n"
    "v_add_f32_dpp v53, v54, v53 wave_shr:1 row_mask:0xf bank_mask:0xf\n"
    "v_mfma_f32_16x16x32_bf16 v[56:59], v[80:83], v[168:171], v[56:59]\n"
    "v_mfma_f32_16x16x32_bf16 v[56:59], v[88:91], v[196:199], v[56:59]\n"
    "v_add_f32_dpp v52, v53, v52 wave_shr:1 row_mask:0xf bank_mask:0xf\n"
    "v_cvt_pk_bf16_f32 v70, v48, v52\n"
    "v_mfma_f32_16x16x32_bf16 v[56:59], v[96:99], v[200:203], v[56:59]\n"
    "v_mfma_f32_16x16x32_bf16 v[56:59], v[104:107], v[204:207], v[56:59]\n"
    "s_waitcnt lgkmcnt(0)\n"
    "buffer_store_dwordx4 v[72:75], v250, s[20:23], 0 offen offset:1024 sc1\n"
    "v_mfma_f32_16x16x32_bf16 v[56:59], v[112:115], v[208:211], v[56:59]\n"
    "s_nop 7\n"
    "v_fmac_f32_dpp v58, v55, v243 wave_ror:1 row_mask:0xf bank_mask:0xf\n"
    "v_add_f32_dpp v58, v59, v58 wave_shr:1 row_mask:0xf bank_mask:0xf\n"
    "v_fmac_f32_dpp v57, v54, v243 wave_ror:1 row_mask:0xf bank_mask:0xf\n"
    "v_fmac_f32_dpp v56, v53, v243 wave_ror:1 row_mask:0xf bank_mask:0xf\n"
    "v_add_f32_dpp v57, v58, v57 wave_shr:1 row_mask:0xf bank_mask:0xf\n"
    "s_nop 1\n"
    "s_nop 0\n"
    "v_add_f32_dpp v56, v57, v56 wave_shr:1 row_mask:0xf bank_mask:0xf\n"
    "v_fmac_f32_dpp v62, v59, v243 wave_ror:1 row_mask:0xf bank_mask:0xf\n"
    "v_add_f32_dpp v62, v63, v62 wave_shr:1 row_mask:0xf bank_mask:0xf\n"
    "v_fmac_f32_dpp v61, v58, v243 wave_ror:1 row_mask:0xf bank_mask:0xf\n"
    "v_fmac_f32_dpp v60, v57, v243 wave_ror:1 row_mask:0xf bank_mask:0xf\n"
    "v_add_f32_dpp v61, v62, v61 wave_shr:1 row_mask:0xf bank_mask:0xf\n"
    "s_nop 1\n"
    "v_add_f32_dpp v60, v61, v60 wave_shr:1 row_mask:0xf bank_mask:0xf\n"
    "v_cvt_pk_bf16_f32 v71, v56, v60\n"
    "ds_write_b128 v251, v[68:71] offset:1024\n"
    "ds_read_b64_tr_b16 v[76:77], v252 offset:1024\n"
    "ds_read_b64_tr_b16 v[78:79], v252 offset:1088\n"
    "s_waitcnt lgkmcnt(0)\n"
    "buffer_store_dwordx4 v[76:79], v250, s[20:23], 0 offen offset:2048 sc1\n"
    ".LZ27_%=:\n"
    "s_endpgm\n"
    ".Lend3_%=:\n"
    :: "{s[0:1]}"(__builtin_amdgcn_kernarg_segment_ptr()), "{s2}"(__builtin_amdgcn_workgroup_id_x()),
       "{v0}"(__builtin_amdgcn_workitem_id_x()),
       "s"((unsigned)(size_t)(__attribute__((address_space(3))) char*)lds)
    : "memory", "vcc", "v255", "s81");
}

extern "C" void kernel_launch(void* const* d_in, const int* in_sizes, int n_in,
                              void* d_out, int out_size, void* d_ws, size_t ws_size,
                              hipStream_t stream) {
  Args a;
  const void* bp[6] = {d_in[0], d_in[10], d_in[11], d_in[12], d_ws, d_out};
  const unsigned bn[6] = {2048u * 512 * 2, 512u * 1376 * 2, 512u * 1376 * 2, 512u * 1376 * 2, 10878976u, 2048u * 512 * 2};
  for (int i = 0; i < 6; i++) { a.b[i].p = bp[i]; a.b[i].n = bn[i]; a.b[i].f = 0x00020000u; }
  a.q[0] = d_in[13]; a.q[1] = d_in[7]; a.q[2] = d_in[8];
  for (int i = 0; i < 6; i++) a.q[3 + i] = d_in[1 + i];
  gmix_kernel<<<256, 512, 0, stream>>>(a);
}
```
